```python
import torch

def _softmax(x, axis=-1):
    return torch.softmax(x, dim=axis)
def _gelu(x):
    return torch.nn.functional.gelu(x, approximate="tanh")
def _silu(x):
    return torch.nn.functional.silu(x)
def _sigmoid(x):
    return torch.sigmoid(x)
def _layernorm(x, axis=-1, eps=1e-5):
    m = x.mean(dim=axis, keepdim=True)
    v = x.var(dim=axis, keepdim=True, unbiased=False)
    return (x - m) / torch.sqrt(v + eps)
def _scatter_add(tgt, idx, src, axis=0):
    return tgt.clone().index_add_(axis, idx, src)
def _segment_sum(src, seg, n):
    out = torch.zeros((n,) + src.shape[1:], dtype=src.dtype)
    return out.index_add_(0, seg, src)
def _pad(x, pw):
    flat = tuple(v for pair in reversed(pw) for v in pair)
    return torch.nn.functional.pad(x, flat)

kernel_name = 'parallel_sum__ssm_conv_s4__dense_activation_mish__prompt'

def setup_inputs():

    _w = (lambda a: (lambda t: t.to(torch.bfloat16) if t.is_floating_point() else t)(torch.as_tensor(a)))
    def _A_setup():
        B, L, D, N = 1, 2048, 576, 128
        A_re = -(lambda _x: torch.exp(torch.as_tensor(_x)))((lambda *s: torch.randn(*s, dtype=torch.bfloat16) * 6.4)(D, N))
        A_im = torch.pi * (lambda *s: torch.randn(*s, dtype=torch.bfloat16) * 0.05)(D, N)
        B_re = (lambda *s: torch.randn(*s, dtype=torch.bfloat16) * 0.1)(D, N)
        B_im = (lambda *s: torch.randn(*s, dtype=torch.bfloat16) * 0.1)(D, N)
        C_re = (lambda *s: torch.randn(*s, dtype=torch.bfloat16) * 0.1)(D, N)
        C_im = (lambda *s: torch.randn(*s, dtype=torch.bfloat16) * 0.1)(D, N)
        log_dt = (lambda *s: torch.randn(*s, dtype=torch.bfloat16) * 0.05)(D) * 0.5 - 4.0
        Dskip = (lambda *s: torch.randn(*s, dtype=torch.bfloat16) * 0.05)(D)
        return {
            "u": _w((lambda *s: torch.randn(*s, dtype=torch.bfloat16) * 0.05)(B, L, D)),
            "A_re": _w(A_re),
            "A_im": _w(A_im),
            "B_re": _w(B_re),
            "B_im": _w(B_im),
            "C_re": _w(C_re),
            "C_im": _w(C_im),
            "log_dt": _w(log_dt),
            "Dskip": _w(Dskip),
        }
    _A_in = _A_setup()

    def _B_setup():
        B, S, D, F = 1, 2048, 576, 4608
        return {
            "x": _w((lambda *s: torch.randn(*s, dtype=torch.bfloat16) * 0.05)(B, S, D)),
            "w1": _w((lambda *s: torch.randn(*s, dtype=torch.bfloat16) * 0.8)(D, F) * (D ** -0.5)),
            "b1": _w((lambda *s: torch.randn(*s, dtype=torch.bfloat16) * 0.2)(F)),
            "w2": _w((lambda *s: torch.randn(*s, dtype=torch.bfloat16) * 0.2)(F, D) * (F ** -0.5)),
        }
    _B_in = _B_setup()

    return {
        "A_u": _A_in["u"],
        "A_A_re": _A_in["A_re"],
        "A_A_im": _A_in["A_im"],
        "A_B_re": _A_in["B_re"],
        "A_B_im": _A_in["B_im"],
        "A_C_re": _A_in["C_re"],
        "A_C_im": _A_in["C_im"],
        "A_log_dt": _A_in["log_dt"],
        "A_Dskip": _A_in["Dskip"],
        "B_x": _B_in["x"],
        "B_w1": _B_in["w1"],
        "B_b1": _B_in["b1"],
        "B_w2": _B_in["w2"],
    }

def reference(A_u, A_A_re, A_A_im, A_B_re, A_B_im, A_C_re, A_C_im, A_log_dt, A_Dskip, B_x, B_w1, B_b1, B_w2):
    def _A_ref(u, A_re, A_im, B_re, B_im, C_re, C_im, log_dt, Dskip):
        Bsz, L, D = u.shape
        dt = torch.exp(log_dt)[:, None]
        A = A_re + 1j * A_im
        Bc = B_re + 1j * B_im
        Cc = C_re + 1j * C_im
        logdA = dt * A
        dB = dt * Bc
        l = (lambda x, dt: x.to(dt))(torch.arange(L), A_re.dtype)
        P = torch.exp(logdA[:, :, None] * l[None, None, :])
        K = torch.einsum("dn,dnl,dn->dl", Cc, P, dB).real
        L2 = 2 * L
        Kp = _pad(K, ((0, 0), (0, L)))
        up = (lambda x, *d: x.permute(*d))(u, 0, 2, 1)
        up = _pad(up, ((0, 0), (0, 0), (0, L)))
        Kf = (lambda x, axes=(-2,-1): torch.fft.rfft2(x.float(), dim=axes))(Kp[:, None, :], axes=(-2, -1))
        Uf = (lambda x, axes=(-2,-1): torch.fft.rfft2(x.float(), dim=axes))(up[:, :, None, :], axes=(-2, -1))
        Yf = Uf * Kf[None, :, :, :]
        y = (lambda x, s, axes=(-2,-1): torch.fft.irfft2(x, s=s, dim=axes).to(torch.bfloat16))(Yf, (1, L2), axes=(-2, -1))[:, :, 0, :L]
        y = (lambda x, *d: x.permute(*d))(y, 0, 2, 1) + u * Dskip[None, None, :]
        return y

    def _B_ref(x, w1, b1, w2):
        h = torch.einsum("bsd,df->bsf", x, w1) + b1
        sp = torch.log1p(torch.exp(-torch.abs(h))) + torch.where(h > 0, h, torch.zeros_like(h))
        act = h * torch.tanh(sp)
        return torch.einsum("bsf,fd->bsd", act, w2)

    x = A_u
    xn = _layernorm(x, axis=-1)
    return x + _A_ref(xn, A_A_re, A_A_im, A_B_re, A_B_im, A_C_re, A_C_im, A_log_dt, A_Dskip) + _B_ref(xn, B_w1, B_b1, B_w2)
```

```cpp
#include <hip/hip_runtime.h>
#include <hip/hip_bf16.h>
#include <stdint.h>

#define ROUND_L 1

constexpr int L = 2048, D = 576, NS = 128, F = 4608;

__device__ __forceinline__ float bf2f(uint16_t v) { return __uint_as_float(((uint32_t)v) << 16); }
__device__ __forceinline__ uint16_t f2bf(float f) {
  uint32_t u = __float_as_uint(f);
  u += 0x7FFF + ((u >> 16) & 1);
  return (uint16_t)(u >> 16);
}
__device__ __forceinline__ float rbf(float f) { return bf2f(f2bf(f)); }

__global__ void ln_kernel(const uint16_t* __restrict__ x, uint16_t* __restrict__ xn) {
  int row = blockIdx.x * 4 + (threadIdx.x >> 6);
  int lane = threadIdx.x & 63;
  const uint16_t* xr = x + (size_t)row * D;
  float v[9];
  float s = 0.f;
  for (int i = 0; i < 9; ++i) { v[i] = bf2f(xr[lane + 64 * i]); s += v[i]; }
  for (int o = 32; o > 0; o >>= 1) s += __shfl_xor(s, o);
  float mean = s * (1.0f / D);
  float q = 0.f;
  for (int i = 0; i < 9; ++i) { float d = v[i] - mean; q += d * d; }
  for (int o = 32; o > 0; o >>= 1) q += __shfl_xor(q, o);
  float var = q * (1.0f / D);
  float mb = rbf(mean), vb = rbf(var);
  float den = rbf(sqrtf(rbf(vb + 1e-5f)));
  for (int i = 0; i < 9; ++i) {
    float d = rbf(v[i] - mb);
    xn[(size_t)row * D + lane + 64 * i] = f2bf(d / den);
  }
}

__global__ void kgen_kernel(const uint16_t* __restrict__ Are, const uint16_t* __restrict__ Aim,
                            const uint16_t* __restrict__ Bre, const uint16_t* __restrict__ Bim,
                            const uint16_t* __restrict__ Cre, const uint16_t* __restrict__ Cim,
                            const uint16_t* __restrict__ logdt, float* __restrict__ K) {
  int d = blockIdx.y;
  int l = blockIdx.x * 256 + threadIdx.x;
  float dt = rbf(expf(bf2f(logdt[d])));
  float lf = rbf((float)l);
  float acc = 0.f;
  for (int n = 0; n < NS; ++n) {
    float ar = dt * bf2f(Are[d * NS + n]);
    float ai = dt * bf2f(Aim[d * NS + n]);
    float br = dt * bf2f(Bre[d * NS + n]);
    float bi = dt * bf2f(Bim[d * NS + n]);
    float cr = bf2f(Cre[d * NS + n]);
    float ci = bf2f(Cim[d * NS + n]);
    float cbr = cr * br - ci * bi;
    float cbi = cr * bi + ci * br;
    float e = expf(ar * lf);
    float sn, cs;
    sincosf(ai * lf, &sn, &cs);
    acc += e * (cbr * cs - cbi * sn);
  }
  K[(size_t)d * L + l] = acc;
}

__global__ void conv_kernel(const uint16_t* __restrict__ xn, const float* __restrict__ K, uint16_t* __restrict__ y) {
  int d = blockIdx.x * 64 + (threadIdx.x & 63);
  int t = blockIdx.y * 4 + (threadIdx.x >> 6);
  float acc = 0.f;
  const float* Kd = K + (size_t)d * L;
  for (int s = 0; s <= t; ++s) acc += Kd[t - s] * bf2f(xn[(size_t)s * D + d]);
  y[(size_t)t * D + d] = f2bf(acc);
}

template <int MODE>
__global__ void gemm_kernel(const uint16_t* __restrict__ A, const uint16_t* __restrict__ B,
                            uint16_t* __restrict__ C, int M, int N, int Kd,
                            const uint16_t* __restrict__ bias, const uint16_t* __restrict__ x,
                            const uint16_t* __restrict__ xn, const uint16_t* __restrict__ y,
                            const uint16_t* __restrict__ Dskip) {
  __shared__ float As[16][65];
  __shared__ float Bs[16][65];
  int tx = threadIdx.x & 15, ty = threadIdx.x >> 4;
  int m0 = blockIdx.y * 64, n0 = blockIdx.x * 64;
  float acc[4][4] = {};
  for (int k0 = 0; k0 < Kd; k0 += 16) {
    for (int i = threadIdx.x; i < 64 * 16; i += 256) {
      int r = i >> 4, c = i & 15;
      As[c][r] = bf2f(A[(size_t)(m0 + r) * Kd + k0 + c]);
    }
    for (int i = threadIdx.x; i < 64 * 16; i += 256) {
      int r = i >> 6, c = i & 63;
      Bs[r][c] = bf2f(B[(size_t)(k0 + r) * N + n0 + c]);
    }
    __syncthreads();
    for (int k = 0; k < 16; ++k) {
      float a[4], b[4];
      for (int i = 0; i < 4; ++i) a[i] = As[k][ty * 4 + i];
      for (int j = 0; j < 4; ++j) b[j] = Bs[k][tx * 4 + j];
      for (int i = 0; i < 4; ++i) for (int j = 0; j < 4; ++j) acc[i][j] += a[i] * b[j];
    }
    __syncthreads();
  }
  for (int i = 0; i < 4; ++i) for (int j = 0; j < 4; ++j) {
    int m = m0 + ty * 4 + i, n = n0 + tx * 4 + j;
    if (MODE == 0) {
      float h = rbf(rbf(acc[i][j]) + bf2f(bias[n]));
      float e = rbf(expf(-fabsf(h)));
      float lp = rbf(log1pf(e));
      float sp = rbf(lp + (h > 0.f ? h : 0.f));
      float th = rbf(tanhf(sp));
      C[(size_t)m * N + n] = f2bf(h * th);
    } else {
      float yb = bf2f(y[(size_t)m * N + n]);
      float ud = rbf(bf2f(xn[(size_t)m * N + n]) * bf2f(Dskip[n]));
      float ya = rbf(yb + ud);
      float o = rbf(bf2f(x[(size_t)m * N + n]) + ya);
      o = o + rbf(acc[i][j]);
      C[(size_t)m * N + n] = f2bf(o);
    }
  }
}

extern "C" void kernel_launch(void* const* d_in, const int* in_sizes, int n_in,
                              void* d_out, int out_size, void* d_ws, size_t ws_size,
                              hipStream_t stream) {
  const uint16_t* x = (const uint16_t*)d_in[0];
  const uint16_t* Are = (const uint16_t*)d_in[1];
  const uint16_t* Aim = (const uint16_t*)d_in[2];
  const uint16_t* Bre = (const uint16_t*)d_in[3];
  const uint16_t* Bim = (const uint16_t*)d_in[4];
  const uint16_t* Cre = (const uint16_t*)d_in[5];
  const uint16_t* Cim = (const uint16_t*)d_in[6];
  const uint16_t* logdt = (const uint16_t*)d_in[7];
  const uint16_t* Dskip = (const uint16_t*)d_in[8];
  const uint16_t* w1 = (const uint16_t*)d_in[10];
  const uint16_t* b1 = (const uint16_t*)d_in[11];
  const uint16_t* w2 = (const uint16_t*)d_in[12];
  uint16_t* out = (uint16_t*)d_out;
  char* ws = (char*)d_ws;
  uint16_t* xn = (uint16_t*)ws;
  uint16_t* y = (uint16_t*)(ws + (4 << 20));
  float* K = (float*)(ws + (8 << 20));
  uint16_t* act = (uint16_t*)(ws + (16 << 20));
  ln_kernel<<<L / 4, 256, 0, stream>>>(x, xn);
  kgen_kernel<<<dim3(L / 256, D), 256, 0, stream>>>(Are, Aim, Bre, Bim, Cre, Cim, logdt, K);
  conv_kernel<<<dim3(D / 64, L / 4), 256, 0, stream>>>(xn, K, y);
  gemm_kernel<0><<<dim3(F / 64, L / 64), 256, 0, stream>>>(xn, w1, act, L, F, D, b1, nullptr, nullptr, nullptr, nullptr);
  gemm_kernel<1><<<dim3(D / 64, L / 64), 256, 0, stream>>>(act, w2, out, L, D, F, nullptr, x, xn, y, Dskip);
}
```

```cpp
#include <hip/hip_runtime.h>
#include <hip/hip_bf16.h>
#include <stdint.h>

typedef unsigned short u16;
typedef __attribute__((ext_vector_type(4))) short s16x4;
typedef __attribute__((ext_vector_type(8))) short s16x8;
typedef __attribute__((ext_vector_type(4))) float f32x4;
typedef __attribute__((ext_vector_type(2))) float f32x2;
typedef __attribute__((ext_vector_type(4))) unsigned u32x4;
typedef __attribute__((ext_vector_type(2))) unsigned u32x2;
typedef __attribute__((ext_vector_type(2))) __bf16 bf16x2_t;
#define LDS_AS __attribute__((address_space(3)))

constexpr int L = 2048, D = 576, NS = 128, F = 4608;
#define STAMP(i)

__device__ __forceinline__ float bf2f(u16 v) { return __uint_as_float(((unsigned)v) << 16); }
__device__ __forceinline__ float bflo(unsigned v) { return __uint_as_float(v << 16); }
__device__ __forceinline__ float bfhi(unsigned v) { return __uint_as_float(v & 0xffff0000u); }
__device__ __forceinline__ unsigned pack_bf16(float a, float b) {
  f32x2 v = {a, b};
  bf16x2_t r = __builtin_convertvector(v, bf16x2_t);
  return __builtin_bit_cast(unsigned, r);
}
__device__ __forceinline__ float rbf(float f) { return __uint_as_float(pack_bf16(f, 0.f) << 16); }

__device__ __forceinline__ void glds16(const void* g, unsigned lds_byte_addr_uniform) {
  __builtin_amdgcn_global_load_lds((const unsigned*)g, (LDS_AS unsigned*)(uintptr_t)lds_byte_addr_uniform, 16, 0, 0);
}
__device__ __forceinline__ s16x4 tr_read(unsigned lds_addr) {
  return __builtin_amdgcn_ds_read_tr16_b64_v4i16((LDS_AS s16x4*)(uintptr_t)lds_addr);
}
__device__ __forceinline__ s16x8 lds_read128(unsigned lds_addr) {
  return *(LDS_AS s16x8*)(uintptr_t)lds_addr;
}
__device__ __forceinline__ s16x4 lds_read64(unsigned lds_addr) {
  return *(LDS_AS s16x4*)(uintptr_t)lds_addr;
}
__device__ __forceinline__ s16x8 cat8(s16x4 a, s16x4 b) { return __builtin_shufflevector(a, b, 0, 1, 2, 3, 4, 5, 6, 7); }

#define RD_TR(dst, addr, off) asm volatile("ds_read_b64_tr_b16 %0, %1 offset:%2" : "=v"(dst) : "v"(addr), "i"(off))
#define RD128(dst, addr, off) asm volatile("ds_read_b128 %0, %1 offset:%2" : "=v"(dst) : "v"(addr), "i"(off))
#define WAIT_WB(n, s) asm volatile("s_waitcnt lgkmcnt(" #n ")" : "+v"(wlo[s]), "+v"(whi[s]))
#define WAIT_WB_XA(n, s, XA) asm volatile("s_waitcnt lgkmcnt(" #n ")" : "+v"(wlo[s]), "+v"(whi[s]), "+v"(XA[0]), "+v"(XA[1]))
#define ISSUE_WB(nt, AE, AO) do { if ((nt) & 1) { RD_TR(wlo[(nt) % 3], AO, (nt) * 32); RD_TR(whi[(nt) % 3], AO, (nt) * 32 + 2304); } \
                                  else { RD_TR(wlo[(nt) % 3], AE, (nt) * 32); RD_TR(whi[(nt) % 3], AE, (nt) * 32 + 2304); } } while (0)
#define MMA4(nt, XA) do { s16x8 wb_ = cat8(wlo[(nt) % 3], whi[(nt) % 3]); \
    _Pragma("unroll") for (int mt = 0; mt < 2; ++mt) acc[nt][mt] = __builtin_amdgcn_mfma_f32_16x16x32_bf16(wb_, XA[mt], acc[nt][mt], 0, 0, 0); } while (0)
#define GSTAGE(XC, XN, AMT, SYNC_CODE, HAVE_NEXT) do { \
    ISSUE_WB(2, cE, cO); WAIT_WB_XA(4, 0, XC); MMA4(0, XC); \
    ISSUE_WB(3, cE, cO); WAIT_WB(4, 1); MMA4(1, XC); \
    ISSUE_WB(4, cE, cO); WAIT_WB(4, 2); MMA4(2, XC); \
    ISSUE_WB(5, cE, cO); WAIT_WB(4, 0); MMA4(3, XC); \
    ISSUE_WB(6, cE, cO); WAIT_WB(4, 1); MMA4(4, XC); \
    ISSUE_WB(7, cE, cO); WAIT_WB(4, 2); MMA4(5, XC); \
    ISSUE_WB(8, cE, cO); WAIT_WB(4, 0); MMA4(6, XC); \
    asm volatile("s_waitcnt lgkmcnt(0)" : "+v"(wlo[1]), "+v"(whi[1]), "+v"(wlo[2]), "+v"(whi[2])); \
    if (HAVE_NEXT) { \
      SYNC_CODE; \
      RD128(XN[0], nA, 0); RD128(XN[1], nA, AMT); \
      ISSUE_WB(0, nE, nO); \
    } \
    MMA4(7, XC); \
    if (HAVE_NEXT) ISSUE_WB(1, nE, nO); \
    MMA4(8, XC); \
  } while (0)
#define VMWAIT2(big, small) do { if (w < 2) asm volatile("s_waitcnt vmcnt(" #big ")" ::: "memory"); else asm volatile("s_waitcnt vmcnt(" #small ")" ::: "memory"); } while (0)

#define RDSET(XS, WL, WH, pA, AMT, pE, pO) do { \
    RD128(XS[0], pA, 0); RD128(XS[1], pA, AMT); \
    _Pragma("unroll") for (int nt_ = 0; nt_ < 9; nt_ += 1) { \
      if (nt_ & 1) { RD_TRX(WL[nt_], pO, nt_ * 32); RD_TRX(WH[nt_], pO, nt_ * 32 + 2304); } \
      else { RD_TRX(WL[nt_], pE, nt_ * 32); RD_TRX(WH[nt_], pE, nt_ * 32 + 2304); } } } while (0)
#define WAITSET(n, XS, WL, WH) asm volatile("s_waitcnt lgkmcnt(" #n ")" : "+v"(XS[0]), "+v"(XS[1]), \
    "+v"(WL[0]), "+v"(WL[1]), "+v"(WL[2]), "+v"(WL[3]), "+v"(WL[4]), "+v"(WL[5]), "+v"(WL[6]), "+v"(WL[7]), "+v"(WL[8]), \
    "+v"(WH[0]), "+v"(WH[1]), "+v"(WH[2]), "+v"(WH[3]), "+v"(WH[4]), "+v"(WH[5]), "+v"(WH[6]), "+v"(WH[7]), "+v"(WH[8]))
#define MMASET(XS, WL, WH) do { _Pragma("unroll") for (int nt_ = 0; nt_ < 9; ++nt_) { s16x8 wb_ = cat8(WL[nt_], WH[nt_]); \
    acc[nt_][0] = __builtin_amdgcn_mfma_f32_16x16x32_bf16(wb_, XS[0], acc[nt_][0], 0, 0, 0); \
    acc[nt_][1] = __builtin_amdgcn_mfma_f32_16x16x32_bf16(wb_, XS[1], acc[nt_][1], 0, 0, 0); } } while (0)
template <int OFF> __device__ __forceinline__ s16x4 rd_tr_t(unsigned addr) { s16x4 r; asm volatile("ds_read_b64_tr_b16 %0, %1 offset:%2" : "=v"(r) : "v"(addr), "i"(OFF)); return r; }
#define RD_TRX(dst, addr, off) dst = rd_tr_sel(addr, off)
__device__ __forceinline__ s16x4 rd_tr_sel(unsigned addr, int off) {
  switch (off) {
#define C_(o) case o: return rd_tr_t<o>(addr);
    C_(0) C_(32) C_(64) C_(96) C_(128) C_(160) C_(192) C_(224) C_(256)
    C_(2304) C_(2336) C_(2368) C_(2400) C_(2432) C_(2464) C_(2496) C_(2528) C_(2560)
#undef C_
  }
  return rd_tr_t<0>(addr);
}

constexpr int LN_ROWS = 16;
constexpr int LN_TS = 592;

struct cplx { float re, im; };
__device__ __forceinline__ cplx cmul(cplx a, cplx b) { return {a.re * b.re - a.im * b.im, a.re * b.im + a.im * b.re}; }
__device__ __forceinline__ cplx csq(cplx a) { return {a.re * a.re - a.im * a.im, 2.f * a.re * a.im}; }
__device__ __forceinline__ cplx cexp_e(float arl2, float air, float e) {
  float m = __builtin_amdgcn_exp2f(arl2 * e);
  float th = air * e;
  th = th - floorf(th);
  float s = __builtin_amdgcn_sinf(th);
  float c = __builtin_amdgcn_cosf(th);
  return {m * c, m * s};
}

constexpr int LN_BLOCKS = L / LN_ROWS;
__global__ __launch_bounds__(512) void ln_kernel(const u16* __restrict__ x, u16* __restrict__ xn, u16* __restrict__ uT,
                                                 int* __restrict__ counters,
    const u16* __restrict__ Are, const u16* __restrict__ Aim,
    const u16* __restrict__ Bre, const u16* __restrict__ Bim, const u16* __restrict__ Cre,
    const u16* __restrict__ Cim, const u16* __restrict__ logdt, u16* __restrict__ KR) {
  __shared__ __attribute__((aligned(16))) char smem[27648];
  const int tid = threadIdx.x, w = tid >> 6, l = tid & 63;
  if (blockIdx.x >= LN_BLOCKS) {
    const int d = blockIdx.x - LN_BLOCKS;
    float* red = (float*)(smem);
    float* Kd = (float*)(smem + 24576);
    {
      const int idx = l & 15, kk = l >> 4;
      const int nb = d * NS + 16 * w + 4 * kk;
      const u32x2 vAr = *(const u32x2*)(Are + nb), vAi = *(const u32x2*)(Aim + nb), vBr = *(const u32x2*)(Bre + nb),
                  vBi = *(const u32x2*)(Bim + nb), vCr = *(const u32x2*)(Cre + nb), vCi = *(const u32x2*)(Cim + nb);
      const float dt = rbf(__expf(bf2f(logdt[d])));
      const float e0 = (float)(128 * (idx >> 3) + (idx & 7));
      const float e1 = idx < 8 ? (float)(256 + 128 * (idx >> 2) + 2 * (idx & 3)) : (float)(512 + 128 * ((idx >> 1) & 3) + 4 * (idx & 1));
      const float e2 = (float)(1024 + 128 * idx);
      const float eb = (float)(8 * idx);
      float av[3][8], bv[8];
#pragma unroll
      for (int i = 0; i < 4; ++i) {
#define SEL_(v) ((i & 1) ? bfhi(v[i >> 1]) : bflo(v[i >> 1]))
        const float ar = dt * SEL_(vAr), ai = dt * SEL_(vAi), br = dt * SEL_(vBr), bi = dt * SEL_(vBi);
        const float cr = SEL_(vCr), ci = SEL_(vCi);
#undef SEL_
        const cplx cb = {cr * br - ci * bi, cr * bi + ci * br};
        const float arl2 = ar * 1.4426950408889634f;
        const float air = ai * 0.15915494309189535f;
        const cplx A0 = cexp_e(arl2, air, e0), A1 = cexp_e(arl2, air, e1), A2 = cexp_e(arl2, air, e2);
        const cplx B = cmul(cb, cexp_e(arl2, air, eb));
        av[0][2 * i] = A0.re; av[0][2 * i + 1] = A0.im;
        av[1][2 * i] = A1.re; av[1][2 * i + 1] = A1.im;
        av[2][2 * i] = A2.re; av[2][2 * i + 1] = A2.im;
        bv[2 * i] = B.re; bv[2 * i + 1] = -B.im;
      }
      u32x4 ah[3], al[3], bh, bl;
#pragma unroll
      for (int j = 0; j < 4; ++j) {
#pragma unroll
        for (int b = 0; b < 3; ++b) {
          const unsigned h = pack_bf16(av[b][2 * j], av[b][2 * j + 1]);
          ah[b][j] = h;
          al[b][j] = pack_bf16(av[b][2 * j] - bflo(h), av[b][2 * j + 1] - bfhi(h));
        }
        const unsigned h = pack_bf16(bv[2 * j], bv[2 * j + 1]);
        bh[j] = h;
        bl[j] = pack_bf16(bv[2 * j] - bflo(h), bv[2 * j + 1] - bfhi(h));
      }
#pragma unroll
      for (int b = 0; b < 3; ++b) {
        f32x4 ka = {0.f, 0.f, 0.f, 0.f};
        ka = __builtin_amdgcn_mfma_f32_16x16x32_bf16(__builtin_bit_cast(s16x8, al[b]), __builtin_bit_cast(s16x8, bh), ka, 0, 0, 0);
        ka = __builtin_amdgcn_mfma_f32_16x16x32_bf16(__builtin_bit_cast(s16x8, ah[b]), __builtin_bit_cast(s16x8, bl), ka, 0, 0, 0);
        ka = __builtin_amdgcn_mfma_f32_16x16x32_bf16(__builtin_bit_cast(s16x8, ah[b]), __builtin_bit_cast(s16x8, bh), ka, 0, 0, 0);
#pragma unroll
        for (int r = 0; r < 4; ++r) red[(w * 3 + b) * 256 + (4 * kk + r) * 16 + idx] = ka[r];
      }
    }
    __syncthreads();
    for (int e = tid; e < 768; e += 512) {
      float sacc = red[e];
#pragma unroll
      for (int ww = 1; ww < 8; ++ww) sacc += red[ww * 768 + e];
      Kd[e] = sacc;
    }
    __syncthreads();
    {
      u32x2 hi2, lo2;
#pragma unroll
      for (int jj = 0; jj < 2; ++jj) {
        float kv[2];
#pragma unroll
        for (int h = 0; h < 2; ++h) {
          const int p = 4 * tid + 2 * jj + h;
          const int ll = 2047 - p;
          const int lr = (int)rbf((float)ll);
          const int q = (lr >> 3) & 15;
          int row;
          if (lr < 256) row = ((lr >> 7) << 3) + (lr & 7);
          else if (lr < 512) row = 16 + (((lr >> 7) & 1) << 2) + ((lr & 7) >> 1);
          else if (lr < 1024) row = 24 + (((lr >> 7) & 3) << 1) + ((lr & 7) >> 2);
          else row = 32 + ((lr - 1024) >> 7);
          kv[h] = Kd[row * 16 + q];
        }
        const unsigned hp = pack_bf16(kv[0], kv[1]);
        hi2[jj] = hp;
        lo2[jj] = pack_bf16(kv[0] - bflo(hp), kv[1] - bfhi(hp));
      }
      *(u32x2*)(KR + (size_t)d * 4096 + 4 * tid) = hi2;
      *(u32x2*)(KR + (size_t)d * 4096 + 2048 + 4 * tid) = lo2;
    }
    return;
  }
  if (blockIdx.x == 0 && tid < 64) counters[tid] = 0;
  const int t0 = blockIdx.x * LN_ROWS;
  const int r = w * 2 + (l >> 5), c = l & 31;
  const bool act_l = c < 24;
  const u32x4* xr = (const u32x4*)(x + (size_t)(t0 + r) * D);
  u32x4 v[3];
#pragma unroll
  for (int i = 0; i < 3; ++i) v[i] = act_l ? xr[c + 24 * i] : (u32x4){0u, 0u, 0u, 0u};
  float s = 0.f;
#pragma unroll
  for (int i = 0; i < 3; ++i)
#pragma unroll
    for (int j = 0; j < 4; ++j) s += bflo(v[i][j]) + bfhi(v[i][j]);
#pragma unroll
  for (int o = 1; o < 32; o <<= 1) s += __shfl_xor(s, o);
  const float mean = s * (1.0f / D);
  float q = 0.f;
#pragma unroll
  for (int i = 0; i < 3; ++i)
#pragma unroll
    for (int j = 0; j < 4; ++j) {
      float a = bflo(v[i][j]) - mean, b = bfhi(v[i][j]) - mean;
      q += a * a + b * b;
    }
  if (!act_l) q = 0.f;
#pragma unroll
  for (int o = 1; o < 32; o <<= 1) q += __shfl_xor(q, o);
  const float var = q * (1.0f / D);
  const float mb = rbf(mean), vb = rbf(var);
  const float den = rbf(sqrtf(rbf(vb + 1e-5f)));
  const float rc = 1.0f / den;
  u32x4* xo = (u32x4*)(xn + (size_t)(t0 + r) * D);
  if (act_l) {
#pragma unroll
    for (int i = 0; i < 3; ++i) {
      u32x4 o;
#pragma unroll
      for (int j = 0; j < 4; ++j) {
        const float da = rbf(bflo(v[i][j]) - mb), db = rbf(bfhi(v[i][j]) - mb);
        float qa = da * rc, qb = db * rc;
        qa = fmaf(fmaf(-qa, den, da), rc, qa);
        qb = fmaf(fmaf(-qb, den, db), rc, qb);
        o[j] = pack_bf16(qa, qb);
      }
      xo[c + 24 * i] = o;
      *(u32x4*)(smem + (r * LN_TS + (c + 24 * i) * 8) * 2) = o;
    }
  }
  __syncthreads();
  const unsigned sbase = (unsigned)(uintptr_t)smem;
  const int g = l >> 4, qq = (l & 15) >> 2, p = l & 3;
  for (int it = w; it < 18; it += 8) {
    const int d0 = it * 32 + (g >> 1) * 16;
    const int rb = (g & 1) * 8;
    s16x4 a = tr_read(sbase + ((rb + qq) * LN_TS + d0 + 4 * p) * 2);
    s16x4 b = tr_read(sbase + ((rb + 4 + qq) * LN_TS + d0 + 4 * p) * 2);
    *(s16x8*)(uT + (size_t)(d0 + (l & 15)) * L + t0 + rb) = cat8(a, b);
  }
}

constexpr int G1_A_BYTES = 128 * 32 * 2;
constexpr int G1_B_BYTES = 32 * 288 * 2;
constexpr int G1_STAGE = G1_A_BYTES + G1_B_BYTES;
constexpr int G1_LDS = 3 * G1_STAGE;

constexpr int CV_UC_STRIDE = 4160;
constexpr int CV_K_OFF = 4 * CV_UC_STRIDE;
constexpr int CV_K_BYTES = 2336 * 2;

__device__ __forceinline__ float mish_f(float h) {
  float t = fminf(h * 1.4426950408889634f, 43.f);
  float e = __builtin_amdgcn_exp2f(t);
  float a = e + 2.f;
  float n = e * a;
  float n2 = fmaf(e, a, 2.f);
  return h * (n * __builtin_amdgcn_rcpf(n2));
}

__global__ __launch_bounds__(512, 3) void gemm1_conv_kernel(
    const u16* __restrict__ xn, const u16* __restrict__ w1, const u16* __restrict__ b1, u16* __restrict__ act,
    const u16* __restrict__ uT, const u16* __restrict__ KR, u16* __restrict__ yT) {
  extern __shared__ __attribute__((aligned(16))) char smem[];
  const unsigned sbase = (unsigned)(uintptr_t)smem;
  const int tid = threadIdx.x, l = tid & 63;
  const int w = __builtin_amdgcn_readfirstlane(tid >> 6);
  const int g = l >> 4, li = l & 15;
  if (blockIdx.x < 256) {
    __builtin_amdgcn_s_setprio(2);
    const int b = blockIdx.x, xcd = b & 7, idx = b >> 3;
    const int mi = 8 * (xcd & 1) + (idx & 7), ni = 4 * (xcd >> 1) + (idx >> 3);
    const int m0 = mi * 128, n0 = ni * 288;
    const int wm = w & 3, wn = w >> 2;
    int aoff, boff[3];
    {
      const int p = w * 64 + l;
      const int row = p >> 2, chp = p & 3;
      const int f = (4 - ((row >> 2) & 3)) & 3;
      aoff = (m0 + row) * D + ((chp ^ f) * 8);
    }
#pragma unroll
    for (int i = 0; i < 3; ++i) {
      int ins = w + 8 * i;
      if (ins >= 18) ins = 0;
      const int p = ins * 64 + l;
      const int row = p / 36, chp = p - row * 36;
      boff[i] = row * F + n0 + ((chp ^ (((row >> 3) & 1) << 1)) * 8);
    }
    auto stage = [&](int t, int buf) {
      const unsigned sb = sbase + buf * G1_STAGE;
      glds16(xn + aoff + t * 32, sb + w * 1024);
      glds16(w1 + boff[0] + (size_t)t * 32 * F, sb + G1_A_BYTES + w * 1024);
      glds16(w1 + boff[1] + (size_t)t * 32 * F, sb + G1_A_BYTES + (w + 8) * 1024);
      if (w < 2) glds16(w1 + boff[2] + (size_t)t * 32 * F, sb + G1_A_BYTES + (w + 16) * 1024);
    };
    const int fa = (4 - ((li >> 2) & 3)) & 3;
    const unsigned a_rd = (32 * wm + li) * 64 + ((g ^ fa) * 16);
    const int q = li >> 2, p4 = li & 3;
    const unsigned b_rd = G1_A_BYTES + (8 * g + q) * 576 + (p4 & 1) * 8;
    const int bsw = (g & 1) << 1;

    f32x4 acc[9][2];
#pragma unroll
    for (int nt = 0; nt < 9; ++nt) {
      const u32x2 bb = *(const u32x2*)(b1 + n0 + wn * 144 + nt * 16 + 4 * g);
      const f32x4 bv = {bflo(bb[0]), bfhi(bb[0]), bflo(bb[1]), bfhi(bb[1])};
#pragma unroll
      for (int mt = 0; mt < 2; ++mt) acc[nt][mt] = bv;
    }
    const unsigned bE0 = b_rd + (p4 >> 1) * 16 + wn * 288 + (wn ? -bsw * 16 : bsw * 16);
    const unsigned bO0 = b_rd + (p4 >> 1) * 16 + wn * 288 + (wn ? bsw * 16 : -bsw * 16);
    s16x4 wlo[3], whi[3];
    s16x8 xa0[2], xa1[2];
    asm volatile("s_waitcnt vmcnt(0)" ::: "memory");
    stage(0, 0);
    stage(1, 1);
    stage(2, 2);
    VMWAIT2(8, 6);
    __builtin_amdgcn_s_barrier();
    unsigned cE = sbase + bE0, cO = sbase + bO0, nA = sbase + a_rd, nE, nO;
    RD128(xa0[0], nA, 0); RD128(xa0[1], nA, 1024);
    ISSUE_WB(0, cE, cO);
    ISSUE_WB(1, cE, cO);
    int buf = 0;
    for (int t = 0; t < 18; t += 2) {
      {
        const int nb = buf == 2 ? 0 : buf + 1;
        const unsigned sbn = sbase + nb * G1_STAGE;
        nA = sbn + a_rd; nE = sbn + bE0; nO = sbn + bO0;
        GSTAGE(xa0, xa1, 1024,
               { if (t + 2 < 18) VMWAIT2(4, 3); else asm volatile("s_waitcnt vmcnt(0)" ::: "memory");
                 __builtin_amdgcn_s_barrier(); if (t + 3 < 18) stage(t + 3, buf); }, true);
        cE = nE; cO = nO; buf = nb;
      }
      {
        const int nb = buf == 2 ? 0 : buf + 1;
        const unsigned sbn = sbase + nb * G1_STAGE;
        nA = sbn + a_rd; nE = sbn + bE0; nO = sbn + bO0;
        const bool hn = t + 2 < 18;
        GSTAGE(xa1, xa0, 1024,
               { if (t + 3 < 18) VMWAIT2(4, 3); else asm volatile("s_waitcnt vmcnt(0)" ::: "memory");
                 __builtin_amdgcn_s_barrier(); if (t + 4 < 18) stage(t + 4, buf); }, hn);
        cE = nE; cO = nO; buf = nb;
      }
    }
#pragma unroll
    for (int mt = 0; mt < 2; ++mt) {
      const int row = 32 * wm + 16 * mt + li;
      const int sw = (row >> 1) & 7;
#pragma unroll
      for (int nt = 0; nt < 9; ++nt) {
        const int n = n0 + wn * 144 + nt * 16 + 4 * g;
        const int kt = n >> 6, ch = (n >> 3) & 7;
        u16* dst = act + ((size_t)(mi * 72 + kt) * 128 + row) * 64 + ((ch ^ sw) * 8) + (n & 7);
        u32x2 o;
        o[0] = pack_bf16(mish_f(acc[nt][mt][0]), mish_f(acc[nt][mt][1]));
        o[1] = pack_bf16(mish_f(acc[nt][mt][2]), mish_f(acc[nt][mt][3]));
        *(u32x2*)(dst) = o;
      }
    }
  } else {
    const int d = blockIdx.x - 256;
    if (tid < 256) {
      const u32x4* up = (const u32x4*)(uT + (size_t)d * L);
      u32x4 a = up[tid];
      u32x4 bnx = (tid < 255) ? up[tid + 1] : (u32x4){0u, 0u, 0u, 0u};
      unsigned wv[6] = {a[0], a[1], a[2], a[3], bnx[0], bnx[1]};
      u32x4 c1, c3;
#pragma unroll
      for (int i = 0; i < 4; ++i) {
        c1[i] = __builtin_amdgcn_alignbyte(wv[i + 1], wv[i], 2);
        c3[i] = __builtin_amdgcn_alignbyte(wv[i + 2], wv[i + 1], 2);
      }
      u32x4 c2 = {wv[1], wv[2], wv[3], wv[4]};
      char* dst = smem + (8 * tid + 16) * 2;
      *(u32x4*)(dst) = a;
      *(u32x4*)(dst + CV_UC_STRIDE) = c1;
      *(u32x4*)(dst + 2 * CV_UC_STRIDE) = c2;
      *(u32x4*)(dst + 3 * CV_UC_STRIDE) = c3;
      if (tid < 64) {
        const int c = tid >> 4, i = tid & 15;
        u16 v = 0;
        if (i + c >= 16) v = uT[(size_t)d * L + i + c - 16];
        *(u16*)(smem + c * CV_UC_STRIDE + i * 2) = v;
      }
    } else {
      const int t2 = tid - 256;
      const u32x4* kp = (const u32x4*)(KR + (size_t)d * 4096);
      *(u32x4*)(smem + CV_K_OFF + t2 * 16) = kp[t2];
      *(u32x4*)(smem + CV_K_OFF + CV_K_BYTES + t2 * 16) = kp[256 + t2];
      if (t2 < 36) {
        *(u32x4*)(smem + CV_K_OFF + 4096 + t2 * 16) = (u32x4){0u, 0u, 0u, 0u};
        *(u32x4*)(smem + CV_K_OFF + CV_K_BYTES + 4096 + t2 * 16) = (u32x4){0u, 0u, 0u, 0u};
      }
    }
    __syncthreads();
    f32x4 acc[8];
#pragma unroll
    for (int j = 0; j < 8; ++j) acc[j] = (f32x4){0.f, 0.f, 0.f, 0.f};
    const int cc = (li + 1) & 3;
    const unsigned ua = sbase + cc * CV_UC_STRIDE + (li + 1 - cc + 8 * g) * 2;
    const unsigned kb = sbase + CV_K_OFF + (2032 - 16 * li + 8 * g) * 2;
    {
      const int rho = w;
      s16x8 A[8], Bh[8], Bl[8];
#pragma unroll
      for (int s = 0; s < 8; ++s) {
        const unsigned ad = ua + (32 * (8 * s + rho)) * 2;
        A[s] = cat8(lds_read64(ad), lds_read64(ad + 8));
      }
#pragma unroll
      for (int dl = 0; dl < 8; ++dl) {
        const unsigned ad = kb + (32 * rho - 256 * dl) * 2;
        Bh[dl] = lds_read128(ad);
        Bl[dl] = lds_read128(ad + CV_K_BYTES);
      }
#pragma unroll
      for (int j = 0; j < 8; ++j)
#pragma unroll
        for (int s = 0; s <= j; ++s) {
          acc[j] = __builtin_amdgcn_mfma_f32_16x16x32_bf16(A[s], Bh[j - s], acc[j], 0, 0, 0);
          acc[j] = __builtin_amdgcn_mfma_f32_16x16x32_bf16(A[s], Bl[j - s], acc[j], 0, 0, 0);
        }
    }
    __syncthreads();
    f32x4* red4 = (f32x4*)smem;
#pragma unroll
    for (int j = 0; j < 8; ++j) red4[(w * 8 + j) * 64 + l] = acc[j];
    __syncthreads();
    {
      const int j = w;
      f32x4 s = red4[j * 64 + l];
#pragma unroll
      for (int ww = 1; ww < 8; ++ww) s += red4[(ww * 8 + j) * 64 + l];
      u32x2 o = {pack_bf16(s[0], s[1]), pack_bf16(s[2], s[3])};
      *(u32x2*)(yT + (size_t)d * L + 256 * j + 16 * li + 4 * g) = o;
    }
  }
}

constexpr int G2_A_BYTES = 128 * 64 * 2;
constexpr int G2_B_BYTES = 32 * 576;
constexpr int G2_STAGE = G2_A_BYTES + G2_B_BYTES;
constexpr int G2_NST = 4;
constexpr int G2_LDS = G2_NST * G2_STAGE;

__global__ __launch_bounds__(512, 2) void gemm2_kernel(
    const u16* __restrict__ act, const u16* __restrict__ w2, float* __restrict__ slabs, int* __restrict__ counters,
    const u16* __restrict__ x, const u16* __restrict__ xn, const u16* __restrict__ yT, const u16* __restrict__ Dskip,
    u16* __restrict__ out) {
  extern __shared__ __attribute__((aligned(16))) char smem[];
  STAMP(0);
  const unsigned sbase = (unsigned)(uintptr_t)smem;
  const int tid = threadIdx.x, l = tid & 63;
  const int w = __builtin_amdgcn_readfirstlane(tid >> 6);
  const int g = l >> 4, li = l & 15;
  const int b = blockIdx.x, xcd = b & 7, idx = b >> 3;
  const int mi = 8 * (xcd & 1) + (idx & 7), ks = xcd >> 1, ni = idx >> 3;
  const int m0 = mi * 128, n0 = ni * 144, k0 = ks * 1152;
  const int wm = w & 3, wk = w >> 2;
  int aoff, boff[3];
  aoff = (mi * 72 + ks * 18) * 8192 + (w * 128 + l) * 8;
#pragma unroll
  for (int i = 0; i < 3; ++i) {
    int ins = w + 8 * i;
    if (ins >= 18) ins = 0;
    const int p = ins * 64 + l;
    const int row = p / 36, chp = p - row * 36;
    const int ch = chp ^ (((row >> 3) & 1) << 1);
    const int half = ch >= 18 ? 1 : 0;
    boff[i] = (k0 + row + 32 * half) * D + n0 + (ch - 18 * half) * 8;
  }
  auto stage = [&](int t, int buf) {
    const unsigned sb = sbase + buf * G2_STAGE;
    glds16(act + aoff + t * 8192, sb + (w * 2) * 1024);
    glds16(act + aoff + t * 8192 + 512, sb + (w * 2 + 1) * 1024);
    glds16(w2 + boff[0] + t * 64 * D, sb + G2_A_BYTES + w * 1024);
    glds16(w2 + boff[1] + t * 64 * D, sb + G2_A_BYTES + (w + 8) * 1024);
    if (w < 2) glds16(w2 + boff[2] + t * 64 * D, sb + G2_A_BYTES + (w + 16) * 1024);
  };
  const unsigned a_rd = (32 * wm + li) * 128 + (((4 * wk + g) ^ ((li >> 1) & 7)) * 16);
  const int q = li >> 2, p4 = li & 3;
  const unsigned b_rd = G2_A_BYTES + (8 * g + q) * 576 + (p4 & 1) * 8;
  const int bsw = (g & 1) << 1;
  f32x4 acc[9][2];
#pragma unroll
  for (int nt = 0; nt < 9; ++nt)
#pragma unroll
    for (int mt = 0; mt < 2; ++mt) acc[nt][mt] = (f32x4){0.f, 0.f, 0.f, 0.f};
  const unsigned bE0 = b_rd + (p4 >> 1) * 16 + wk * 288 + (wk ? -bsw * 16 : bsw * 16);
  const unsigned bO0 = b_rd + (p4 >> 1) * 16 + wk * 288 + (wk ? bsw * 16 : -bsw * 16);
  s16x8 xa0[2], xa1[2];
  s16x4 wl0[9], wh0[9], wl1[9], wh1[9];
  stage(0, 0);
  stage(1, 1);
  stage(2, 2);
  VMWAIT2(10, 8);
  __builtin_amdgcn_s_barrier();
  STAMP(1);
  {
    const unsigned pA = sbase + a_rd, pE = sbase + bE0, pO = sbase + bO0;
    RDSET(xa0, wl0, wh0, pA, 2048, pE, pO);
  }
  for (int t = 0; t < 16; t += 2) {
    {
      VMWAIT2(5, 4);
      __builtin_amdgcn_s_barrier();
      stage(t + 3, (t + 3) & 3);
      const unsigned sbn = sbase + ((t + 1) & 3) * G2_STAGE;
      const unsigned pA = sbn + a_rd, pE = sbn + bE0, pO = sbn + bO0;
      WAITSET(0, xa0, wl0, wh0);
      RDSET(xa1, wl1, wh1, pA, 2048, pE, pO);
      MMASET(xa0, wl0, wh0);
    }
    {
      VMWAIT2(5, 4);
      __builtin_amdgcn_s_barrier();
      if (t + 4 < 18) stage(t + 4, (t + 4) & 3);
      const unsigned sbn = sbase + ((t + 2) & 3) * G2_STAGE;
      const unsigned pA = sbn + a_rd, pE = sbn + bE0, pO = sbn + bO0;
      WAITSET(0, xa1, wl1, wh1);
      RDSET(xa0, wl0, wh0, pA, 2048, pE, pO);
      MMASET(xa1, wl1, wh1);
    }
  }
  {
    asm volatile("s_waitcnt vmcnt(0)" ::: "memory");
    __builtin_amdgcn_s_barrier();
    const unsigned sbn = sbase + (17 & 3) * G2_STAGE;
    const unsigned pA = sbn + a_rd, pE = sbn + bE0, pO = sbn + bO0;
    WAITSET(0, xa0, wl0, wh0);
    RDSET(xa1, wl1, wh1, pA, 2048, pE, pO);
    MMASET(xa0, wl0, wh0);
    WAITSET(0, xa1, wl1, wh1);
    MMASET(xa1, wl1, wh1);
  }
  __syncthreads();
  STAMP(2);
  {
    f32x4* ex = (f32x4*)smem;
#pragma unroll
    for (int nt = 0; nt < 9; ++nt) ex[(w * 9 + nt) * 64 + l] = wk ? acc[nt][0] : acc[nt][1];
    __syncthreads();
    const int pw = w ^ 4;
#pragma unroll
    for (int nt = 0; nt < 9; ++nt) {
      f32x4 v = ex[(pw * 9 + nt) * 64 + l];
      f32x4 mine = wk ? acc[nt][1] : acc[nt][0];
      acc[nt][0] = mine + v;
    }
  }
  const int grp = mi * 4 + ni;
  {
    float* gslab = slabs + (size_t)grp * 4 * 4 * 2 * 9 * 64 * 4;
    auto rs = __builtin_amdgcn_make_buffer_rsrc((void*)gslab, 0, 4 * 4 * 2 * 9 * 64 * 16, 0x00020000);
    __syncthreads();
    f32x4* own = (f32x4*)smem;
    if (wm == ks) {
#pragma unroll
      for (int nt = 0; nt < 9; ++nt) own[(wk * 9 + nt) * 64 + l] = acc[nt][0];
    } else {
#pragma unroll
      for (int nt = 0; nt < 9; ++nt) {
        const int off = ((((wm * 4 + ks) * 2 + wk) * 9 + nt) * 64 + l) * 16;
        __builtin_amdgcn_raw_buffer_store_b128(__builtin_bit_cast(u32x4, acc[nt][0]), rs, off, 0, 16);
      }
    }
    asm volatile("s_waitcnt vmcnt(0)" ::: "memory");
    __syncthreads();
    STAMP(3);
    if (tid == 0) {
      __hip_atomic_fetch_add(&counters[grp], 1, __ATOMIC_RELAXED, __HIP_MEMORY_SCOPE_AGENT);
    }
    const int R0 = m0 + 32 * ks;
    u32x2 xv[3], nv[3], dv[3];
    u16 yv[3][4];
#pragma unroll
    for (int i = 0; i < 3; ++i) {
      const int tl = w + 8 * i;
      if (tl < 18) {
        const int h = tl / 9, nt = tl - 9 * h;
        const int row = R0 + 16 * h + li, col = n0 + nt * 16 + 4 * g;
        const size_t go = (size_t)row * D + col;
        xv[i] = *(const u32x2*)(x + go);
        nv[i] = *(const u32x2*)(xn + go);
        dv[i] = *(const u32x2*)(Dskip + col);
#pragma unroll
        for (int e = 0; e < 4; ++e) yv[i][e] = yT[(size_t)(col + e) * L + row];
      }
    }
    if (tid == 0) {
      for (int spin = 0; spin < (1 << 22); ++spin) {
        if (__hip_atomic_load(&counters[grp], __ATOMIC_RELAXED, __HIP_MEMORY_SCOPE_AGENT) >= 4) break;
        __builtin_amdgcn_s_sleep(1);
      }
    }
    __syncthreads();
    STAMP(4);
    u32x4 pv[3][3];
#pragma unroll
    for (int i = 0; i < 3; ++i) {
      const int tl = w + 8 * i;
      if (tl < 18) {
#pragma unroll
        for (int k = 0; k < 3; ++k) {
          const int src = k + (k >= ks ? 1 : 0);
          const int off = (((ks * 4 + src) * 18 + tl) * 64 + l) * 16;
          pv[i][k] = __builtin_amdgcn_raw_buffer_load_b128(rs, off, 0, 16);
        }
      }
    }
#pragma unroll
    for (int i = 0; i < 3; ++i) {
      const int tl = w + 8 * i;
      if (tl < 18) {
        const int h = tl / 9, nt = tl - 9 * h;
        const int row = R0 + 16 * h + li, col = n0 + nt * 16 + 4 * g;
        f32x4 parts[4];
        const f32x4 mine = own[tl * 64 + l];
#pragma unroll
        for (int k = 0; k < 4; ++k) {
          const int kk = k - (k > ks ? 1 : 0);
          parts[k] = (k == ks) ? mine : __builtin_bit_cast(f32x4, pv[i][kk < 3 ? kk : 2]);
        }
        const f32x4 sum = (parts[0] + parts[1]) + (parts[2] + parts[3]);
        float o[4];
#pragma unroll
        for (int e = 0; e < 4; ++e) {
          const float xf = (e & 1) ? bfhi(xv[i][e >> 1]) : bflo(xv[i][e >> 1]);
          const float nf = (e & 1) ? bfhi(nv[i][e >> 1]) : bflo(nv[i][e >> 1]);
          const float df = (e & 1) ? bfhi(dv[i][e >> 1]) : bflo(dv[i][e >> 1]);
          const float ya = rbf(bf2f(yv[i][e]) + rbf(nf * df));
          o[e] = rbf(xf + ya) + rbf(sum[e]);
        }
        u32x2 ov = {pack_bf16(o[0], o[1]), pack_bf16(o[2], o[3])};
        *(u32x2*)(out + (size_t)row * D + col) = ov;
      }
    }
  }
}

extern "C" void kernel_launch(void* const* d_in, const int* in_sizes, int n_in,
                              void* d_out, int out_size, void* d_ws, size_t ws_size,
                              hipStream_t stream) {
  const u16* x = (const u16*)d_in[0];
  const u16* Are = (const u16*)d_in[1];
  const u16* Aim = (const u16*)d_in[2];
  const u16* Bre = (const u16*)d_in[3];
  const u16* Bim = (const u16*)d_in[4];
  const u16* Cre = (const u16*)d_in[5];
  const u16* Cim = (const u16*)d_in[6];
  const u16* logdt = (const u16*)d_in[7];
  const u16* Dskip = (const u16*)d_in[8];
  const u16* w1 = (const u16*)d_in[10];
  const u16* b1 = (const u16*)d_in[11];
  const u16* w2 = (const u16*)d_in[12];
  u16* out = (u16*)d_out;
  char* ws = (char*)d_ws;
  u16* xn = (u16*)(ws);
  u16* uT = (u16*)(ws + (4 << 20));
  u16* yT = (u16*)(ws + (8 << 20));
  u16* KR = (u16*)(ws + (12 << 20));
  int* counters = (int*)(ws + (20 << 20));
  u16* act = (u16*)(ws + (24 << 20));
  float* slabs = (float*)(ws + (48 << 20));
  hipFuncSetAttribute((const void*)gemm1_conv_kernel, hipFuncAttributeMaxDynamicSharedMemorySize, G1_LDS);
  hipFuncSetAttribute((const void*)gemm2_kernel, hipFuncAttributeMaxDynamicSharedMemorySize, G2_LDS);
  ln_kernel<<<LN_BLOCKS + D, 512, 0, stream>>>(x, xn, uT, counters, Are, Aim, Bre, Bim, Cre, Cim, logdt, KR);
  gemm1_conv_kernel<<<256 + D, 512, G1_LDS, stream>>>(xn, w1, b1, act, uT, KR, yT);
  gemm2_kernel<<<256, 512, G2_LDS, stream>>>(act, w2, slabs, counters, x, xn, yT, Dskip, out);
}
```

```cpp
#include <hip/hip_runtime.h>
#include <hip/hip_bf16.h>
#include <stdint.h>

typedef unsigned short u16;
typedef __attribute__((ext_vector_type(4))) short s16x4;
typedef __attribute__((ext_vector_type(8))) short s16x8;
typedef __attribute__((ext_vector_type(4))) float f32x4;
typedef __attribute__((ext_vector_type(2))) float f32x2;
typedef __attribute__((ext_vector_type(4))) unsigned u32x4;
typedef __attribute__((ext_vector_type(2))) unsigned u32x2;
typedef __attribute__((ext_vector_type(2))) __bf16 bf16x2_t;
#define LDS_AS __attribute__((address_space(3)))

constexpr int L = 2048, D = 576, NS = 128, F = 4608;
#define STAMP(i)

__device__ __forceinline__ float bf2f(u16 v) { return __uint_as_float(((unsigned)v) << 16); }
__device__ __forceinline__ float bflo(unsigned v) { return __uint_as_float(v << 16); }
__device__ __forceinline__ float bfhi(unsigned v) { return __uint_as_float(v & 0xffff0000u); }
__device__ __forceinline__ unsigned pack_bf16(float a, float b) {
  f32x2 v = {a, b};
  bf16x2_t r = __builtin_convertvector(v, bf16x2_t);
  return __builtin_bit_cast(unsigned, r);
}
__device__ __forceinline__ float rbf(float f) { return __uint_as_float(pack_bf16(f, 0.f) << 16); }

__device__ __forceinline__ void glds16(const void* g, unsigned lds_byte_addr_uniform) {
  __builtin_amdgcn_global_load_lds((const unsigned*)g, (LDS_AS unsigned*)(uintptr_t)lds_byte_addr_uniform, 16, 0, 0);
}
__device__ __forceinline__ s16x4 tr_read(unsigned lds_addr) {
  return __builtin_amdgcn_ds_read_tr16_b64_v4i16((LDS_AS s16x4*)(uintptr_t)lds_addr);
}
__device__ __forceinline__ s16x8 lds_read128(unsigned lds_addr) {
  return *(LDS_AS s16x8*)(uintptr_t)lds_addr;
}
__device__ __forceinline__ s16x4 lds_read64(unsigned lds_addr) {
  return *(LDS_AS s16x4*)(uintptr_t)lds_addr;
}
__device__ __forceinline__ s16x8 cat8(s16x4 a, s16x4 b) { return __builtin_shufflevector(a, b, 0, 1, 2, 3, 4, 5, 6, 7); }

#define RD_TR(dst, addr, off) asm volatile("ds_read_b64_tr_b16 %0, %1 offset:%2" : "=v"(dst) : "v"(addr), "i"(off))
#define RD128(dst, addr, off) asm volatile("ds_read_b128 %0, %1 offset:%2" : "=v"(dst) : "v"(addr), "i"(off))
#define WAIT_WB(n, s) asm volatile("s_waitcnt lgkmcnt(" #n ")" : "+v"(wlo[s]), "+v"(whi[s]))
#define WAIT_WB_XA(n, s, XA) asm volatile("s_waitcnt lgkmcnt(" #n ")" : "+v"(wlo[s]), "+v"(whi[s]), "+v"(XA[0]), "+v"(XA[1]))
#define ISSUE_WB(nt, AE, AO) do { if ((nt) & 1) { RD_TR(wlo[(nt) % 3], AO, (nt) * 32); RD_TR(whi[(nt) % 3], AO, (nt) * 32 + 2304); } \
                                  else { RD_TR(wlo[(nt) % 3], AE, (nt) * 32); RD_TR(whi[(nt) % 3], AE, (nt) * 32 + 2304); } } while (0)
#define MMA4(nt, XA) do { s16x8 wb_ = cat8(wlo[(nt) % 3], whi[(nt) % 3]); \
    _Pragma("unroll") for (int mt = 0; mt < 2; ++mt) acc[nt][mt] = __builtin_amdgcn_mfma_f32_16x16x32_bf16(wb_, XA[mt], acc[nt][mt], 0, 0, 0); } while (0)
#define GSTAGE(XC, XN, AMT, SYNC_CODE, HAVE_NEXT) do { \
    ISSUE_WB(2, cE, cO); WAIT_WB_XA(4, 0, XC); MMA4(0, XC); \
    ISSUE_WB(3, cE, cO); WAIT_WB(4, 1); MMA4(1, XC); \
    ISSUE_WB(4, cE, cO); WAIT_WB(4, 2); MMA4(2, XC); \
    ISSUE_WB(5, cE, cO); WAIT_WB(4, 0); MMA4(3, XC); \
    ISSUE_WB(6, cE, cO); WAIT_WB(4, 1); MMA4(4, XC); \
    ISSUE_WB(7, cE, cO); WAIT_WB(4, 2); MMA4(5, XC); \
    ISSUE_WB(8, cE, cO); WAIT_WB(4, 0); MMA4(6, XC); \
    asm volatile("s_waitcnt lgkmcnt(0)" : "+v"(wlo[1]), "+v"(whi[1]), "+v"(wlo[2]), "+v"(whi[2])); \
    if (HAVE_NEXT) { \
      SYNC_CODE; \
      RD128(XN[0], nA, 0); RD128(XN[1], nA, AMT); \
      ISSUE_WB(0, nE, nO); \
    } \
    MMA4(7, XC); \
    if (HAVE_NEXT) ISSUE_WB(1, nE, nO); \
    MMA4(8, XC); \
  } while (0)
#define VMWAIT2(big, small) do { if (w < 2) asm volatile("s_waitcnt vmcnt(" #big ")" ::: "memory"); else asm volatile("s_waitcnt vmcnt(" #small ")" ::: "memory"); } while (0)

#define RDSET(XS, WL, WH, pA, AMT, pE, pO) do { \
    RD128(XS[0], pA, 0); RD128(XS[1], pA, AMT); \
    _Pragma("unroll") for (int nt_ = 0; nt_ < 9; nt_ += 1) { \
      if (nt_ & 1) { RD_TRX(WL[nt_], pO, nt_ * 32); RD_TRX(WH[nt_], pO, nt_ * 32 + 2304); } \
      else { RD_TRX(WL[nt_], pE, nt_ * 32); RD_TRX(WH[nt_], pE, nt_ * 32 + 2304); } } } while (0)
#define WAITSET(n, XS, WL, WH) asm volatile("s_waitcnt lgkmcnt(" #n ")" : "+v"(XS[0]), "+v"(XS[1]), \
    "+v"(WL[0]), "+v"(WL[1]), "+v"(WL[2]), "+v"(WL[3]), "+v"(WL[4]), "+v"(WL[5]), "+v"(WL[6]), "+v"(WL[7]), "+v"(WL[8]), \
    "+v"(WH[0]), "+v"(WH[1]), "+v"(WH[2]), "+v"(WH[3]), "+v"(WH[4]), "+v"(WH[5]), "+v"(WH[6]), "+v"(WH[7]), "+v"(WH[8]))
#define MMASET(XS, WL, WH) do { _Pragma("unroll") for (int nt_ = 0; nt_ < 9; ++nt_) { s16x8 wb_ = cat8(WL[nt_], WH[nt_]); \
    acc[nt_][0] = __builtin_amdgcn_mfma_f32_16x16x32_bf16(wb_, XS[0], acc[nt_][0], 0, 0, 0); \
    acc[nt_][1] = __builtin_amdgcn_mfma_f32_16x16x32_bf16(wb_, XS[1], acc[nt_][1], 0, 0, 0); } } while (0)
template <int OFF> __device__ __forceinline__ s16x4 rd_tr_t(unsigned addr) { s16x4 r; asm volatile("ds_read_b64_tr_b16 %0, %1 offset:%2" : "=v"(r) : "v"(addr), "i"(OFF)); return r; }
#define RD_TRX(dst, addr, off) dst = rd_tr_sel(addr, off)
__device__ __forceinline__ s16x4 rd_tr_sel(unsigned addr, int off) {
  switch (off) {
#define C_(o) case o: return rd_tr_t<o>(addr);
    C_(0) C_(32) C_(64) C_(96) C_(128) C_(160) C_(192) C_(224) C_(256)
    C_(2304) C_(2336) C_(2368) C_(2400) C_(2432) C_(2464) C_(2496) C_(2528) C_(2560)
#undef C_
  }
  return rd_tr_t<0>(addr);
}

constexpr int LN_ROWS = 16;
constexpr int LN_TS = 592;

struct cplx { float re, im; };
__device__ __forceinline__ cplx cmul(cplx a, cplx b) { return {a.re * b.re - a.im * b.im, a.re * b.im + a.im * b.re}; }
__device__ __forceinline__ cplx csq(cplx a) { return {a.re * a.re - a.im * a.im, 2.f * a.re * a.im}; }
__device__ __forceinline__ cplx cexp_e(float arl2, float air, float e) {
  float m = __builtin_amdgcn_exp2f(arl2 * e);
  float th = air * e;
  th = th - floorf(th);
  float s = __builtin_amdgcn_sinf(th);
  float c = __builtin_amdgcn_cosf(th);
  return {m * c, m * s};
}

constexpr int LN_BLOCKS = L / LN_ROWS;
__global__ __launch_bounds__(512) void ln_kernel(const u16* __restrict__ x, u16* __restrict__ xn, u16* __restrict__ uT,
                                                 int* __restrict__ counters,
    const u16* __restrict__ Are, const u16* __restrict__ Aim,
    const u16* __restrict__ Bre, const u16* __restrict__ Bim, const u16* __restrict__ Cre,
    const u16* __restrict__ Cim, const u16* __restrict__ logdt, u16* __restrict__ KR, float* __restrict__ Ksc,
    const u16* __restrict__ w1, const u16* __restrict__ w2, unsigned* __restrict__ sink) {
  __shared__ __attribute__((aligned(16))) char smem[27648];
  const int tid = threadIdx.x, w = tid >> 6, l = tid & 63;
  if (blockIdx.x >= LN_BLOCKS + D) {
    const int p = blockIdx.x - (LN_BLOCKS + D), xq = (p & 7) >> 1, idx = p >> 3;
    u32x4 v1[6], v2[6];
    const u32x4* w2p = (const u32x4*)(w2 + (size_t)(1152 * xq + 36 * idx) * D);
#pragma unroll
    for (int j = 0; j < 6; ++j) {
      int i = tid + 512 * j;
      if (i >= 18 * 144) i = tid;
      const int r = i / 144, c = i - r * 144;
      v1[j] = *(const u32x4*)(w1 + (size_t)(18 * idx + r) * F + 1152 * xq + c * 8);
      v2[j] = w2p[i];
    }
    u32x4 accv = {0u, 0u, 0u, 0u};
#pragma unroll
    for (int j = 0; j < 6; ++j) accv ^= v1[j] ^ v2[j];
    const unsigned v = accv[0] ^ accv[1] ^ accv[2] ^ accv[3];
    if (v == 0x9e3779b9u) sink[tid] = v;
    return;
  }
  const int bx = blockIdx.x;
  if (bx >= LN_BLOCKS) {
    const int d = bx - LN_BLOCKS;
    float* red = (float*)(smem);
    float* Kd = (float*)(smem + 24576);
    {
      const int idx = l & 15, kk = l >> 4;
      const int nb = d * NS + 16 * w + 4 * kk;
      const u32x2 vAr = *(const u32x2*)(Are + nb), vAi = *(const u32x2*)(Aim + nb), vBr = *(const u32x2*)(Bre + nb),
                  vBi = *(const u32x2*)(Bim + nb), vCr = *(const u32x2*)(Cre + nb), vCi = *(const u32x2*)(Cim + nb);
      const float dt = rbf(__expf(bf2f(logdt[d])));
      const float e0 = (float)(128 * (idx >> 3) + (idx & 7));
      const float e1 = idx < 8 ? (float)(256 + 128 * (idx >> 2) + 2 * (idx & 3)) : (float)(512 + 128 * ((idx >> 1) & 3) + 4 * (idx & 1));
      const float e2 = (float)(1024 + 128 * idx);
      const float eb = (float)(8 * idx);
      float av[3][8], bv[8];
#pragma unroll
      for (int i = 0; i < 4; ++i) {
#define SEL_(v) ((i & 1) ? bfhi(v[i >> 1]) : bflo(v[i >> 1]))
        const float ar = dt * SEL_(vAr), ai = dt * SEL_(vAi), br = dt * SEL_(vBr), bi = dt * SEL_(vBi);
        const float cr = SEL_(vCr), ci = SEL_(vCi);
#undef SEL_
        const cplx cb = {cr * br - ci * bi, cr * bi + ci * br};
        const float arl2 = ar * 1.4426950408889634f;
        const float air = ai * 0.15915494309189535f;
        const cplx A0 = cexp_e(arl2, air, e0), A1 = cexp_e(arl2, air, e1), A2 = cexp_e(arl2, air, e2);
        const cplx B = cmul(cb, cexp_e(arl2, air, eb));
        av[0][2 * i] = A0.re; av[0][2 * i + 1] = A0.im;
        av[1][2 * i] = A1.re; av[1][2 * i + 1] = A1.im;
        av[2][2 * i] = A2.re; av[2][2 * i + 1] = A2.im;
        bv[2 * i] = B.re; bv[2 * i + 1] = -B.im;
      }
      u32x4 ah[3], al[3], bh, bl;
#pragma unroll
      for (int j = 0; j < 4; ++j) {
#pragma unroll
        for (int b = 0; b < 3; ++b) {
          const unsigned h = pack_bf16(av[b][2 * j], av[b][2 * j + 1]);
          ah[b][j] = h;
          al[b][j] = pack_bf16(av[b][2 * j] - bflo(h), av[b][2 * j + 1] - bfhi(h));
        }
        const unsigned h = pack_bf16(bv[2 * j], bv[2 * j + 1]);
        bh[j] = h;
        bl[j] = pack_bf16(bv[2 * j] - bflo(h), bv[2 * j + 1] - bfhi(h));
      }
#pragma unroll
      for (int b = 0; b < 3; ++b) {
        f32x4 ka = {0.f, 0.f, 0.f, 0.f};
        ka = __builtin_amdgcn_mfma_f32_16x16x32_bf16(__builtin_bit_cast(s16x8, al[b]), __builtin_bit_cast(s16x8, bh), ka, 0, 0, 0);
        ka = __builtin_amdgcn_mfma_f32_16x16x32_bf16(__builtin_bit_cast(s16x8, ah[b]), __builtin_bit_cast(s16x8, bl), ka, 0, 0, 0);
        ka = __builtin_amdgcn_mfma_f32_16x16x32_bf16(__builtin_bit_cast(s16x8, ah[b]), __builtin_bit_cast(s16x8, bh), ka, 0, 0, 0);
#pragma unroll
        for (int r = 0; r < 4; ++r) red[(w * 3 + b) * 256 + (4 * kk + r) * 16 + idx] = ka[r];
      }
    }
    __syncthreads();
    for (int e = tid; e < 768; e += 512) {
      float sacc = red[e];
#pragma unroll
      for (int ww = 1; ww < 8; ++ww) sacc += red[ww * 768 + e];
      Kd[e] = sacc;
    }
    __syncthreads();
    {
      float kv[4];
      float mx = 0.f;
#pragma unroll
      for (int h = 0; h < 4; ++h) {
        const int p = 4 * tid + h;
        const int ll = 2047 - p;
        const int lr = (int)rbf((float)ll);
        const int q = (lr >> 3) & 15;
        int row;
        if (lr < 256) row = ((lr >> 7) << 3) + (lr & 7);
        else if (lr < 512) row = 16 + (((lr >> 7) & 1) << 2) + ((lr & 7) >> 1);
        else if (lr < 1024) row = 24 + (((lr >> 7) & 3) << 1) + ((lr & 7) >> 2);
        else row = 32 + ((lr - 1024) >> 7);
        kv[h] = Kd[row * 16 + q];
        mx = fmaxf(mx, fabsf(kv[h]));
      }
#pragma unroll
      for (int o = 1; o < 64; o <<= 1) mx = fmaxf(mx, __shfl_xor(mx, o));
      float* wmx = red;
      if (l == 0) wmx[w] = mx;
      __syncthreads();
      mx = wmx[0];
#pragma unroll
      for (int ww = 1; ww < 8; ++ww) mx = fmaxf(mx, wmx[ww]);
      int ex = (int)((__float_as_uint(mx) >> 23) & 255u);
      int se = 137 - ex;
      se = se > 120 ? 120 : (se < -120 ? -120 : se);
      const float sc = __uint_as_float((unsigned)(127 + se) << 23);
      typedef __attribute__((ext_vector_type(2))) _Float16 h16x2;
      h16x2 p0 = {(_Float16)(kv[0] * sc), (_Float16)(kv[1] * sc)};
      h16x2 p1 = {(_Float16)(kv[2] * sc), (_Float16)(kv[3] * sc)};
      u32x2 o2 = {__builtin_bit_cast(unsigned, p0), __builtin_bit_cast(unsigned, p1)};
      *(u32x2*)(KR + (size_t)d * 2048 + 4 * tid) = o2;
      if (tid == 0) Ksc[d] = __uint_as_float((unsigned)(127 - se) << 23);
    }
    return;
  }
  if (bx == 0 && tid < 64) counters[tid] = 0;
  const int t0 = bx * LN_ROWS;
  const int r = w * 2 + (l >> 5), c = l & 31;
  const bool act_l = c < 24;
  const u32x4* xr = (const u32x4*)(x + (size_t)(t0 + r) * D);
  u32x4 v[3];
#pragma unroll
  for (int i = 0; i < 3; ++i) v[i] = act_l ? xr[c + 24 * i] : (u32x4){0u, 0u, 0u, 0u};
  float s = 0.f;
#pragma unroll
  for (int i = 0; i < 3; ++i)
#pragma unroll
    for (int j = 0; j < 4; ++j) s += bflo(v[i][j]) + bfhi(v[i][j]);
#pragma unroll
  for (int o = 1; o < 32; o <<= 1) s += __shfl_xor(s, o);
  const float mean = s * (1.0f / D);
  float q = 0.f;
#pragma unroll
  for (int i = 0; i < 3; ++i)
#pragma unroll
    for (int j = 0; j < 4; ++j) {
      float a = bflo(v[i][j]) - mean, b = bfhi(v[i][j]) - mean;
      q += a * a + b * b;
    }
  if (!act_l) q = 0.f;
#pragma unroll
  for (int o = 1; o < 32; o <<= 1) q += __shfl_xor(q, o);
  const float var = q * (1.0f / D);
  const float mb = rbf(mean), vb = rbf(var);
  const float den = rbf(sqrtf(rbf(vb + 1e-5f)));
  const float rc = 1.0f / den;
  u32x4* xo = (u32x4*)(xn + (size_t)(t0 + r) * D);
  if (act_l) {
#pragma unroll
    for (int i = 0; i < 3; ++i) {
      u32x4 o;
#pragma unroll
      for (int j = 0; j < 4; ++j) {
        const float da = rbf(bflo(v[i][j]) - mb), db = rbf(bfhi(v[i][j]) - mb);
        float qa = da * rc, qb = db * rc;
        qa = fmaf(fmaf(-qa, den, da), rc, qa);
        qb = fmaf(fmaf(-qb, den, db), rc, qb);
        o[j] = pack_bf16(qa, qb);
      }
      xo[c + 24 * i] = o;
      *(u32x4*)(smem + (r * LN_TS + (c + 24 * i) * 8) * 2) = o;
    }
  }
  __syncthreads();
  const unsigned sbase = (unsigned)(uintptr_t)smem;
  const int g = l >> 4, qq = (l & 15) >> 2, p = l & 3;
  for (int it = w; it < 18; it += 8) {
    const int d0 = it * 32 + (g >> 1) * 16;
    const int rb = (g & 1) * 8;
    s16x4 a = tr_read(sbase + ((rb + qq) * LN_TS + d0 + 4 * p) * 2);
    s16x4 b = tr_read(sbase + ((rb + 4 + qq) * LN_TS + d0 + 4 * p) * 2);
    *(s16x8*)(uT + (size_t)(d0 + (l & 15)) * L + t0 + rb) = cat8(a, b);
  }
}

constexpr int G1_A_BYTES = 128 * 32 * 2;
constexpr int G1_B_BYTES = 32 * 288 * 2;
constexpr int G1_STAGE = G1_A_BYTES + G1_B_BYTES;
constexpr int G1_LDS = 4 * G1_STAGE;

constexpr int CV_UC_STRIDE = 4160;
constexpr int CV_K_OFF = 4 * CV_UC_STRIDE;
constexpr int CV_K_BYTES = 2336 * 2;
constexpr int CV_KD_OFF = CV_K_OFF + 2 * CV_K_BYTES;
constexpr int CV_CH_STRIDE = 32768;
constexpr int CV_BLOCKS = D / 3;

__device__ __forceinline__ float mish_f(float h) {
  float t = fminf(h * 1.4426950408889634f, 43.f);
  float e = __builtin_amdgcn_exp2f(t);
  float a = e + 2.f;
  float n = e * a;
  float n2 = fmaf(e, a, 2.f);
  return h * (n * __builtin_amdgcn_rcpf(n2));
}

__device__ __forceinline__ unsigned mish2(float h0, float h1) {
  const f32x2 h = {h0, h1};
  f32x2 t = h * 1.4426950408889634f;
  f32x2 e = {__builtin_amdgcn_exp2f(fminf(t[0], 43.f)), __builtin_amdgcn_exp2f(fminf(t[1], 43.f))};
  f32x2 a = e + 2.f;
  f32x2 n = e * a;
  f32x2 n2 = n + 2.f;
  f32x2 r = {__builtin_amdgcn_rcpf(n2[0]), __builtin_amdgcn_rcpf(n2[1])};
  f32x2 o = h * (n * r);
  return pack_bf16(o[0], o[1]);
}

__global__ __launch_bounds__(768, 3) void gemm1_conv_kernel(
    const u16* __restrict__ xn, const u16* __restrict__ w1, const u16* __restrict__ b1, u16* __restrict__ act,
    const u16* __restrict__ uT, const u16* __restrict__ KR, const float* __restrict__ Ksc, u16* __restrict__ yT) {
  extern __shared__ __attribute__((aligned(16))) char smem[];
  const unsigned sbase = (unsigned)(uintptr_t)smem;
  const int tid = threadIdx.x, l = tid & 63;
  const int w = __builtin_amdgcn_readfirstlane(tid >> 6);
  const int g = l >> 4, li = l & 15;
  if (blockIdx.x < 256) {
    const int b = blockIdx.x, xcd = b & 7, idx = b >> 3;
    const int mi = 8 * (xcd & 1) + (idx & 7), ni = 4 * (xcd >> 1) + (idx >> 3);
    const int m0 = mi * 128, n0 = ni * 288;
    if (w >= 8) {
      const int lw = w - 8;
      int aoff[2], boff[5];
#pragma unroll
      for (int j = 0; j < 2; ++j) {
        const int p = (lw + 4 * j) * 64 + l;
        const int row = p >> 2, chp = p & 3;
        const int f = (4 - ((row >> 2) & 3)) & 3;
        aoff[j] = (m0 + row) * D + ((chp ^ f) * 8);
      }
#pragma unroll
      for (int j = 0; j < 5; ++j) {
        int ins = lw + 4 * j;
        if (ins >= 18) ins = 0;
        const int p = ins * 64 + l;
        const int row = p / 36, chp = p - row * 36;
        boff[j] = row * F + n0 + ((chp ^ (((row >> 3) & 1) << 1)) * 8);
      }
      auto stage = [&](int t, int buf) {
        const unsigned sb = sbase + buf * G1_STAGE;
#pragma unroll
        for (int j = 0; j < 2; ++j) glds16(xn + aoff[j] + t * 32, sb + (lw + 4 * j) * 1024);
#pragma unroll
        for (int j = 0; j < 4; ++j) glds16(w1 + boff[j] + (size_t)t * 32 * F, sb + G1_A_BYTES + (lw + 4 * j) * 1024);
        if (lw < 2) glds16(w1 + boff[4] + (size_t)t * 32 * F, sb + G1_A_BYTES + (lw + 16) * 1024);
      };
#define LVMWAIT(big, small) do { if (lw < 2) asm volatile("s_waitcnt vmcnt(" #big ")" ::: "memory"); else asm volatile("s_waitcnt vmcnt(" #small ")" ::: "memory"); } while (0)
      stage(0, 0);
      stage(1, 1);
      stage(2, 2);
      LVMWAIT(14, 12);
      __builtin_amdgcn_s_barrier();
      for (int t = 0; t < 17; ++t) {
        if (t + 2 < 18) LVMWAIT(7, 6); else asm volatile("s_waitcnt vmcnt(0)" ::: "memory");
        __builtin_amdgcn_s_barrier();
        if (t + 3 < 18) stage(t + 3, (t + 3) & 3);
      }
    } else {
      const int wm = w & 3, wn = w >> 2;
      const int fa = (4 - ((li >> 2) & 3)) & 3;
      const unsigned a_rd = (32 * wm + li) * 64 + ((g ^ fa) * 16);
      const int q = li >> 2, p4 = li & 3;
      const unsigned b_rd = G1_A_BYTES + (8 * g + q) * 576 + (p4 & 1) * 8;
      const int bsw = (g & 1) << 1;
      f32x4 acc[9][2];
#pragma unroll
      for (int nt = 0; nt < 9; ++nt) {
        const u32x2 bb = *(const u32x2*)(b1 + n0 + wn * 144 + nt * 16 + 4 * g);
        const f32x4 bv = {bflo(bb[0]), bfhi(bb[0]), bflo(bb[1]), bfhi(bb[1])};
#pragma unroll
        for (int mt = 0; mt < 2; ++mt) acc[nt][mt] = bv;
      }
      const unsigned bE0 = b_rd + (p4 >> 1) * 16 + wn * 288 + (wn ? -bsw * 16 : bsw * 16);
      const unsigned bO0 = b_rd + (p4 >> 1) * 16 + wn * 288 + (wn ? bsw * 16 : -bsw * 16);
      s16x8 xa0[2], xa1[2];
      s16x4 wl0[9], wh0[9], wl1[9], wh1[9];
      __builtin_amdgcn_s_barrier();
      {
        const unsigned pA = sbase + a_rd, pE = sbase + bE0, pO = sbase + bO0;
        RDSET(xa0, wl0, wh0, pA, 1024, pE, pO);
      }
      for (int t = 0; t < 16; t += 2) {
        {
          __builtin_amdgcn_s_barrier();
          const unsigned sbn = sbase + ((t + 1) & 3) * G1_STAGE;
          const unsigned pA = sbn + a_rd, pE = sbn + bE0, pO = sbn + bO0;
          WAITSET(0, xa0, wl0, wh0);
          RDSET(xa1, wl1, wh1, pA, 1024, pE, pO);
          MMASET(xa0, wl0, wh0);
        }
        {
          __builtin_amdgcn_s_barrier();
          const unsigned sbn = sbase + ((t + 2) & 3) * G1_STAGE;
          const unsigned pA = sbn + a_rd, pE = sbn + bE0, pO = sbn + bO0;
          WAITSET(0, xa1, wl1, wh1);
          RDSET(xa0, wl0, wh0, pA, 1024, pE, pO);
          MMASET(xa1, wl1, wh1);
        }
      }
      {
        __builtin_amdgcn_s_barrier();
        const unsigned sbn = sbase + (17 & 3) * G1_STAGE;
        const unsigned pA = sbn + a_rd, pE = sbn + bE0, pO = sbn + bO0;
        WAITSET(0, xa0, wl0, wh0);
        RDSET(xa1, wl1, wh1, pA, 1024, pE, pO);
        MMASET(xa0, wl0, wh0);
        WAITSET(0, xa1, wl1, wh1);
        MMASET(xa1, wl1, wh1);
      }
      int tz = threadIdx.x;
      asm volatile("" : "+v"(tz));
      const int li2 = tz & 15, g2 = (tz >> 4) & 3;
#pragma unroll
      for (int mt = 0; mt < 2; ++mt) {
        const int row = 32 * wm + 16 * mt + li2;
        const int sw = (row >> 1) & 7;
#pragma unroll
        for (int nt = 0; nt < 9; ++nt) {
          const int n = n0 + wn * 144 + nt * 16 + 4 * g2;
          const int kt = n >> 6, ch = (n >> 3) & 7;
          u16* dst = act + ((size_t)(mi * 72 + kt) * 128 + row) * 64 + ((ch ^ sw) * 8) + (n & 7);
          u32x2 o;
          o[0] = mish2(acc[nt][mt][0], acc[nt][mt][1]);
          o[1] = mish2(acc[nt][mt][2], acc[nt][mt][3]);
          *(u32x2*)(dst) = o;
          if (nt % 3 == 2) __builtin_amdgcn_sched_barrier(0);
        }
      }
    }
  } else {
    typedef __attribute__((ext_vector_type(8))) _Float16 h16x8;
    typedef __attribute__((ext_vector_type(2))) _Float16 h16x2;
    const int ch = w >> 2, wc = w & 3, tc = tid & 255;
    const int d = (blockIdx.x - 256) * 3 + ch;
    char* cs = smem + ch * CV_CH_STRIDE;
    const unsigned cbase = sbase + ch * CV_CH_STRIDE;
    {
      const u32x4* up = (const u32x4*)(uT + (size_t)d * L);
      const u32x4 ua_ = up[tc];
      const u32x2 ub_ = (tc < 255) ? *(const u32x2*)(up + tc + 1) : (u32x2){0u, 0u};
      const u32x4 kv = *(const u32x4*)(KR + (size_t)d * 2048 + 8 * tc);
      *(u32x4*)(cs + CV_K_OFF + tc * 16) = kv;
      if (tc < 36) *(u32x4*)(cs + CV_K_OFF + 4096 + tc * 16) = (u32x4){0u, 0u, 0u, 0u};
      unsigned wv[6];
#pragma unroll
      for (int i = 0; i < 6; ++i) {
        const unsigned src = i < 4 ? ua_[i] : ub_[i - 4];
        h16x2 hv = {(_Float16)bflo(src), (_Float16)bfhi(src)};
        wv[i] = __builtin_bit_cast(unsigned, hv);
      }
      u32x4 c0 = {wv[0], wv[1], wv[2], wv[3]}, c1, c3;
#pragma unroll
      for (int i = 0; i < 4; ++i) {
        c1[i] = __builtin_amdgcn_alignbyte(wv[i + 1], wv[i], 2);
        c3[i] = __builtin_amdgcn_alignbyte(wv[i + 2], wv[i + 1], 2);
      }
      u32x4 c2 = {wv[1], wv[2], wv[3], wv[4]};
      char* dst = cs + (8 * tc + 16) * 2;
      *(u32x4*)(dst) = c0;
      *(u32x4*)(dst + CV_UC_STRIDE) = c1;
      *(u32x4*)(dst + 2 * CV_UC_STRIDE) = c2;
      *(u32x4*)(dst + 3 * CV_UC_STRIDE) = c3;
      if (tc < 64) {
        const int c = tc >> 4, i = tc & 15;
        _Float16 v = (_Float16)0.f;
        if (i + c >= 16) v = (_Float16)bf2f(uT[(size_t)d * L + i + c - 16]);
        *(_Float16*)(cs + c * CV_UC_STRIDE + i * 2) = v;
      }
    }
    const float ysc = Ksc[d];
    __syncthreads();
    f32x4 acc[8];
#pragma unroll
    for (int j = 0; j < 8; ++j) acc[j] = (f32x4){0.f, 0.f, 0.f, 0.f};
    const int cc = (li + 1) & 3;
    const unsigned ua = cbase + cc * CV_UC_STRIDE + (li + 1 - cc + 8 * g) * 2;
    const unsigned kb = cbase + CV_K_OFF + (2032 - 16 * li + 8 * g) * 2;
#pragma unroll
    for (int rr = 0; rr < 2; ++rr) {
      const int rho = 2 * wc + rr;
      s16x8 A[8], Bh[8];
#pragma unroll
      for (int s = 0; s < 8; ++s) {
        const unsigned ad = ua + (32 * (8 * s + rho)) * 2;
        A[s] = cat8(lds_read64(ad), lds_read64(ad + 8));
      }
#pragma unroll
      for (int dl = 0; dl < 8; ++dl) Bh[dl] = lds_read128(kb + (32 * rho - 256 * dl) * 2);
#pragma unroll
      for (int j = 0; j < 8; ++j)
#pragma unroll
        for (int s = 0; s <= j; ++s)
          acc[j] = __builtin_amdgcn_mfma_f32_16x16x32_f16(__builtin_bit_cast(h16x8, A[s]), __builtin_bit_cast(h16x8, Bh[j - s]), acc[j], 0, 0, 0);
    }
    __syncthreads();
    f32x4* red4 = (f32x4*)cs;
#pragma unroll
    for (int j = 0; j < 8; ++j) red4[(wc * 8 + j) * 64 + l] = acc[j];
    __syncthreads();
#pragma unroll
    for (int jj = 0; jj < 2; ++jj) {
      const int j = 2 * wc + jj;
      f32x4 sv = (red4[j * 64 + l] + red4[(8 + j) * 64 + l]) + (red4[(16 + j) * 64 + l] + red4[(24 + j) * 64 + l]);
      sv *= ysc;
      u32x2 o = {pack_bf16(sv[0], sv[1]), pack_bf16(sv[2], sv[3])};
      *(u32x2*)(yT + (size_t)d * L + 256 * j + 16 * li + 4 * g) = o;
    }
  }
}

constexpr int G2_A_BYTES = 128 * 64 * 2;
constexpr int G2_B_BYTES = 32 * 576;
constexpr int G2_STAGE = G2_A_BYTES + G2_B_BYTES;
constexpr int G2_NST = 4;
constexpr int G2_LDS = G2_NST * G2_STAGE;

__global__ __launch_bounds__(768, 3) void gemm2_kernel(
    const u16* __restrict__ act, const u16* __restrict__ w2, float* __restrict__ slabs, int* __restrict__ counters,
    const u16* __restrict__ x, const u16* __restrict__ xn, const u16* __restrict__ yT, const u16* __restrict__ Dskip,
    u16* __restrict__ out) {
  extern __shared__ __attribute__((aligned(16))) char smem[];
  STAMP(0);
  const unsigned sbase = (unsigned)(uintptr_t)smem;
  const int tid = threadIdx.x, l = tid & 63;
  const int w = __builtin_amdgcn_readfirstlane(tid >> 6);
  const int g = l >> 4, li = l & 15;
  const int b = blockIdx.x, xcd = b & 7, idx = b >> 3;
  const int mi = 8 * (xcd & 1) + (idx & 7), ks = xcd >> 1, ni = idx >> 3;
  const int m0 = mi * 128, n0 = ni * 144, k0 = ks * 1152;
  const int wm = w & 3, wk = (w >> 2) & 1;
  f32x4 acc[9][2];
#pragma unroll
  for (int nt = 0; nt < 9; ++nt)
#pragma unroll
    for (int mt = 0; mt < 2; ++mt) acc[nt][mt] = (f32x4){0.f, 0.f, 0.f, 0.f};
  if (w >= 8) {
    const int lw = w - 8;
    const u16* aptr = act + (size_t)(mi * 72 + ks * 18) * 8192 + (lw * 64 + l) * 8;
    int boff[5];
#pragma unroll
    for (int j = 0; j < 5; ++j) {
      int ins = lw + 4 * j;
      if (ins >= 18) ins = 0;
      const int p = ins * 64 + l;
      const int row = p / 36, chp = p - row * 36;
      const int ch = chp ^ (((row >> 3) & 1) << 1);
      const int half = ch >= 18 ? 1 : 0;
      boff[j] = (k0 + row + 32 * half) * D + n0 + (ch - 18 * half) * 8;
    }
    auto stage = [&](int t, int buf) {
      const unsigned sb = sbase + buf * G2_STAGE;
#pragma unroll
      for (int j = 0; j < 4; ++j) glds16(aptr + t * 8192 + j * 2048, sb + (lw + 4 * j) * 1024);
#pragma unroll
      for (int j = 0; j < 4; ++j) glds16(w2 + boff[j] + t * 64 * D, sb + G2_A_BYTES + (lw + 4 * j) * 1024);
      if (lw < 2) glds16(w2 + boff[4] + t * 64 * D, sb + G2_A_BYTES + (lw + 16) * 1024);
    };
    stage(0, 0);
    stage(1, 1);
    stage(2, 2);
    LVMWAIT(18, 16);
    __builtin_amdgcn_s_barrier();
    for (int t = 0; t < 17; ++t) {
      if (t + 2 < 18) LVMWAIT(9, 8); else asm volatile("s_waitcnt vmcnt(0)" ::: "memory");
      __builtin_amdgcn_s_barrier();
      if (t + 3 < 18) stage(t + 3, (t + 3) & 3);
    }
  } else {
    const unsigned a_rd = (32 * wm + li) * 128 + (((4 * wk + g) ^ ((li >> 1) & 7)) * 16);
    const int q = li >> 2, p4 = li & 3;
    const unsigned b_rd = G2_A_BYTES + (8 * g + q) * 576 + (p4 & 1) * 8;
    const int bsw = (g & 1) << 1;
    const unsigned bE0 = b_rd + (p4 >> 1) * 16 + wk * 288 + (wk ? -bsw * 16 : bsw * 16);
    const unsigned bO0 = b_rd + (p4 >> 1) * 16 + wk * 288 + (wk ? bsw * 16 : -bsw * 16);
    s16x8 xa0[2], xa1[2];
    s16x4 wl0[9], wh0[9], wl1[9], wh1[9];
    __builtin_amdgcn_s_barrier();
    STAMP(1);
    {
      const unsigned pA = sbase + a_rd, pE = sbase + bE0, pO = sbase + bO0;
      RDSET(xa0, wl0, wh0, pA, 2048, pE, pO);
    }
    for (int t = 0; t < 16; t += 2) {
      {
        __builtin_amdgcn_s_barrier();
        const unsigned sbn = sbase + ((t + 1) & 3) * G2_STAGE;
        const unsigned pA = sbn + a_rd, pE = sbn + bE0, pO = sbn + bO0;
        WAITSET(0, xa0, wl0, wh0);
        RDSET(xa1, wl1, wh1, pA, 2048, pE, pO);
        MMASET(xa0, wl0, wh0);
      }
      {
        __builtin_amdgcn_s_barrier();
        const unsigned sbn = sbase + ((t + 2) & 3) * G2_STAGE;
        const unsigned pA = sbn + a_rd, pE = sbn + bE0, pO = sbn + bO0;
        WAITSET(0, xa1, wl1, wh1);
        RDSET(xa0, wl0, wh0, pA, 2048, pE, pO);
        MMASET(xa1, wl1, wh1);
      }
    }
    {
      __builtin_amdgcn_s_barrier();
      const unsigned sbn = sbase + (17 & 3) * G2_STAGE;
      const unsigned pA = sbn + a_rd, pE = sbn + bE0, pO = sbn + bO0;
      WAITSET(0, xa0, wl0, wh0);
      RDSET(xa1, wl1, wh1, pA, 2048, pE, pO);
      MMASET(xa0, wl0, wh0);
      WAITSET(0, xa1, wl1, wh1);
      MMASET(xa1, wl1, wh1);
    }
  }
  __syncthreads();
  STAMP(2);
  f32x4 res[9];
  {
    f32x4* ex = (f32x4*)smem;
    if (w < 4) {
#pragma unroll
      for (int nt = 0; nt < 9; ++nt) ex[(w * 9 + nt) * 64 + l] = acc[nt][1];
    } else if (w < 8) {
#pragma unroll
      for (int nt = 0; nt < 9; ++nt) ex[(w * 9 + nt) * 64 + l] = acc[nt][0];
    }
    __syncthreads();
    const int pw = (w ^ 4) & 7;
    if (w < 4) {
#pragma unroll
      for (int nt = 0; nt < 9; ++nt) res[nt] = acc[nt][0] + ex[(pw * 9 + nt) * 64 + l];
    } else {
#pragma unroll
      for (int nt = 0; nt < 9; ++nt) res[nt] = acc[nt][1] + ex[(pw * 9 + nt) * 64 + l];
    }
  }
  const int grp = mi * 4 + ni;
  {
    float* gslab = slabs + (size_t)grp * 4 * 4 * 2 * 9 * 64 * 4;
    auto rs = __builtin_amdgcn_make_buffer_rsrc((void*)gslab, 0, 4 * 4 * 2 * 9 * 64 * 16, 0x00020000);
    __syncthreads();
    f32x4* own = (f32x4*)smem;
    if (w >= 8) {
    } else if (wm == ks) {
#pragma unroll
      for (int nt = 0; nt < 9; ++nt) own[(wk * 9 + nt) * 64 + l] = res[nt];
    } else {
#pragma unroll
      for (int nt = 0; nt < 9; ++nt) {
        const int off = ((((wm * 4 + ks) * 2 + wk) * 9 + nt) * 64 + l) * 16;
        __builtin_amdgcn_raw_buffer_store_b128(__builtin_bit_cast(u32x4, res[nt]), rs, off, 0, 16);
      }
    }
    asm volatile("s_waitcnt vmcnt(0)" ::: "memory");
    __syncthreads();
    STAMP(3);
    if (tid == 0) {
      __hip_atomic_fetch_add(&counters[grp], 1, __ATOMIC_RELAXED, __HIP_MEMORY_SCOPE_AGENT);
    }
    const int R0 = m0 + 32 * ks;
    u32x2 xv[2], nv[2], dv[2];
    u16 yv[2][4];
#pragma unroll
    for (int i = 0; i < 2; ++i) {
      const int tl = w + 12 * i;
      if (tl < 18) {
        const int h = tl / 9, nt = tl - 9 * h;
        const int row = R0 + 16 * h + li, col = n0 + nt * 16 + 4 * g;
        const size_t go = (size_t)row * D + col;
        xv[i] = *(const u32x2*)(x + go);
        nv[i] = *(const u32x2*)(xn + go);
        dv[i] = *(const u32x2*)(Dskip + col);
#pragma unroll
        for (int e = 0; e < 4; ++e) yv[i][e] = yT[(size_t)(col + e) * L + row];
      }
    }
    if (tid == 0) {
      for (int spin = 0; spin < (1 << 22); ++spin) {
        if (__hip_atomic_load(&counters[grp], __ATOMIC_RELAXED, __HIP_MEMORY_SCOPE_AGENT) >= 4) break;
        __builtin_amdgcn_s_sleep(1);
      }
    }
    __syncthreads();
    STAMP(4);
    u32x4 pv[2][3];
#pragma unroll
    for (int i = 0; i < 2; ++i) {
      const int tl = w + 12 * i;
      if (tl < 18) {
#pragma unroll
        for (int k = 0; k < 3; ++k) {
          const int src = k + (k >= ks ? 1 : 0);
          const int off = (((ks * 4 + src) * 18 + tl) * 64 + l) * 16;
          pv[i][k] = __builtin_amdgcn_raw_buffer_load_b128(rs, off, 0, 16);
        }
      }
    }
#pragma unroll
    for (int i = 0; i < 2; ++i) {
      const int tl = w + 12 * i;
      if (tl < 18) {
        const int h = tl / 9, nt = tl - 9 * h;
        const int row = R0 + 16 * h + li, col = n0 + nt * 16 + 4 * g;
        f32x4 parts[4];
        const f32x4 mine = own[tl * 64 + l];
#pragma unroll
        for (int k = 0; k < 4; ++k) {
          const int kk = k - (k > ks ? 1 : 0);
          parts[k] = (k == ks) ? mine : __builtin_bit_cast(f32x4, pv[i][kk < 3 ? kk : 2]);
        }
        const f32x4 sum = (parts[0] + parts[1]) + (parts[2] + parts[3]);
        float o[4];
#pragma unroll
        for (int e = 0; e < 4; ++e) {
          const float xf = (e & 1) ? bfhi(xv[i][e >> 1]) : bflo(xv[i][e >> 1]);
          const float nf = (e & 1) ? bfhi(nv[i][e >> 1]) : bflo(nv[i][e >> 1]);
          const float df = (e & 1) ? bfhi(dv[i][e >> 1]) : bflo(dv[i][e >> 1]);
          const float ya = rbf(bf2f(yv[i][e]) + rbf(nf * df));
          o[e] = rbf(xf + ya) + rbf(sum[e]);
        }
        u32x2 ov = {pack_bf16(o[0], o[1]), pack_bf16(o[2], o[3])};
        *(u32x2*)(out + (size_t)row * D + col) = ov;
      }
    }
  }
}

extern "C" void kernel_launch(void* const* d_in, const int* in_sizes, int n_in,
                              void* d_out, int out_size, void* d_ws, size_t ws_size,
                              hipStream_t stream) {
  const u16* x = (const u16*)d_in[0];
  const u16* Are = (const u16*)d_in[1];
  const u16* Aim = (const u16*)d_in[2];
  const u16* Bre = (const u16*)d_in[3];
  const u16* Bim = (const u16*)d_in[4];
  const u16* Cre = (const u16*)d_in[5];
  const u16* Cim = (const u16*)d_in[6];
  const u16* logdt = (const u16*)d_in[7];
  const u16* Dskip = (const u16*)d_in[8];
  const u16* w1 = (const u16*)d_in[10];
  const u16* b1 = (const u16*)d_in[11];
  const u16* w2 = (const u16*)d_in[12];
  u16* out = (u16*)d_out;
  char* ws = (char*)d_ws;
  u16* xn = (u16*)(ws);
  u16* uT = (u16*)(ws + (4 << 20));
  u16* yT = (u16*)(ws + (8 << 20));
  u16* KR = (u16*)(ws + (12 << 20));
  int* counters = (int*)(ws + (20 << 20));
  float* Ksc = (float*)(ws + (21 << 20));
  u16* act = (u16*)(ws + (24 << 20));
  float* slabs = (float*)(ws + (48 << 20));
  hipFuncSetAttribute((const void*)gemm1_conv_kernel, hipFuncAttributeMaxDynamicSharedMemorySize, G1_LDS);
  hipFuncSetAttribute((const void*)gemm2_kernel, hipFuncAttributeMaxDynamicSharedMemorySize, G2_LDS);
  ln_kernel<<<LN_BLOCKS + D + 256, 512, 0, stream>>>(x, xn, uT, counters, Are, Aim, Bre, Bim, Cre, Cim, logdt, KR, Ksc, w1, w2, (unsigned*)(ws + (22 << 20)));
  gemm1_conv_kernel<<<256 + CV_BLOCKS, 768, G1_LDS, stream>>>(xn, w1, b1, act, uT, KR, Ksc, yT);
  gemm2_kernel<<<256, 768, G2_LDS, stream>>>(act, w2, slabs, counters, x, xn, yT, Dskip, out);
}
```

```cpp
#include <hip/hip_runtime.h>
#include <hip/hip_bf16.h>
#include <stdint.h>

typedef unsigned short u16;
typedef __attribute__((ext_vector_type(4))) short s16x4;
typedef __attribute__((ext_vector_type(8))) short s16x8;
typedef __attribute__((ext_vector_type(4))) float f32x4;
typedef __attribute__((ext_vector_type(2))) float f32x2;
typedef __attribute__((ext_vector_type(4))) unsigned u32x4;
typedef __attribute__((ext_vector_type(2))) unsigned u32x2;
typedef __attribute__((ext_vector_type(2))) __bf16 bf16x2_t;
#define LDS_AS __attribute__((address_space(3)))

constexpr int L = 2048, D = 576, NS = 128, F = 4608;
#define STAMP(i)

__device__ __forceinline__ float bf2f(u16 v) { return __uint_as_float(((unsigned)v) << 16); }
__device__ __forceinline__ float bflo(unsigned v) { return __uint_as_float(v << 16); }
__device__ __forceinline__ float bfhi(unsigned v) { return __uint_as_float(v & 0xffff0000u); }
__device__ __forceinline__ unsigned pack_bf16(float a, float b) {
  f32x2 v = {a, b};
  bf16x2_t r = __builtin_convertvector(v, bf16x2_t);
  return __builtin_bit_cast(unsigned, r);
}
__device__ __forceinline__ float rbf(float f) { return __uint_as_float(pack_bf16(f, 0.f) << 16); }

__device__ __forceinline__ void glds16(const void* g, unsigned lds_byte_addr_uniform) {
  __builtin_amdgcn_global_load_lds((const unsigned*)g, (LDS_AS unsigned*)(uintptr_t)lds_byte_addr_uniform, 16, 0, 0);
}
__device__ __forceinline__ s16x4 tr_read(unsigned lds_addr) {
  return __builtin_amdgcn_ds_read_tr16_b64_v4i16((LDS_AS s16x4*)(uintptr_t)lds_addr);
}
__device__ __forceinline__ s16x8 lds_read128(unsigned lds_addr) {
  return *(LDS_AS s16x8*)(uintptr_t)lds_addr;
}
__device__ __forceinline__ s16x4 lds_read64(unsigned lds_addr) {
  return *(LDS_AS s16x4*)(uintptr_t)lds_addr;
}
__device__ __forceinline__ s16x8 cat8(s16x4 a, s16x4 b) { return __builtin_shufflevector(a, b, 0, 1, 2, 3, 4, 5, 6, 7); }

#define RD_TR(dst, addr, off) asm volatile("ds_read_b64_tr_b16 %0, %1 offset:%2" : "=v"(dst) : "v"(addr), "i"(off))
#define RD128(dst, addr, off) asm volatile("ds_read_b128 %0, %1 offset:%2" : "=v"(dst) : "v"(addr), "i"(off))
#define WAIT_WB(n, s) asm volatile("s_waitcnt lgkmcnt(" #n ")" : "+v"(wlo[s]), "+v"(whi[s]))
#define WAIT_WB_XA(n, s, XA) asm volatile("s_waitcnt lgkmcnt(" #n ")" : "+v"(wlo[s]), "+v"(whi[s]), "+v"(XA[0]), "+v"(XA[1]))
#define ISSUE_WB(nt, AE, AO) do { if ((nt) & 1) { RD_TR(wlo[(nt) % 3], AO, (nt) * 32); RD_TR(whi[(nt) % 3], AO, (nt) * 32 + 2304); } \
                                  else { RD_TR(wlo[(nt) % 3], AE, (nt) * 32); RD_TR(whi[(nt) % 3], AE, (nt) * 32 + 2304); } } while (0)
#define MMA4(nt, XA) do { s16x8 wb_ = cat8(wlo[(nt) % 3], whi[(nt) % 3]); \
    _Pragma("unroll") for (int mt = 0; mt < 2; ++mt) acc[nt][mt] = __builtin_amdgcn_mfma_f32_16x16x32_bf16(wb_, XA[mt], acc[nt][mt], 0, 0, 0); } while (0)
#define GSTAGE(XC, XN, AMT, SYNC_CODE, HAVE_NEXT) do { \
    ISSUE_WB(2, cE, cO); WAIT_WB_XA(4, 0, XC); MMA4(0, XC); \
    ISSUE_WB(3, cE, cO); WAIT_WB(4, 1); MMA4(1, XC); \
    ISSUE_WB(4, cE, cO); WAIT_WB(4, 2); MMA4(2, XC); \
    ISSUE_WB(5, cE, cO); WAIT_WB(4, 0); MMA4(3, XC); \
    ISSUE_WB(6, cE, cO); WAIT_WB(4, 1); MMA4(4, XC); \
    ISSUE_WB(7, cE, cO); WAIT_WB(4, 2); MMA4(5, XC); \
    ISSUE_WB(8, cE, cO); WAIT_WB(4, 0); MMA4(6, XC); \
    asm volatile("s_waitcnt lgkmcnt(0)" : "+v"(wlo[1]), "+v"(whi[1]), "+v"(wlo[2]), "+v"(whi[2])); \
    if (HAVE_NEXT) { \
      SYNC_CODE; \
      RD128(XN[0], nA, 0); RD128(XN[1], nA, AMT); \
      ISSUE_WB(0, nE, nO); \
    } \
    MMA4(7, XC); \
    if (HAVE_NEXT) ISSUE_WB(1, nE, nO); \
    MMA4(8, XC); \
  } while (0)
#define VMWAIT2(big, small) do { if (w < 2) asm volatile("s_waitcnt vmcnt(" #big ")" ::: "memory"); else asm volatile("s_waitcnt vmcnt(" #small ")" ::: "memory"); } while (0)

#define RDSET(XS, WL, WH, pA, AMT, pE, pO) do { \
    RD128(XS[0], pA, 0); RD128(XS[1], pA, AMT); \
    _Pragma("unroll") for (int nt_ = 0; nt_ < 9; nt_ += 1) { \
      if (nt_ & 1) { RD_TRX(WL[nt_], pO, nt_ * 32); RD_TRX(WH[nt_], pO, nt_ * 32 + 2304); } \
      else { RD_TRX(WL[nt_], pE, nt_ * 32); RD_TRX(WH[nt_], pE, nt_ * 32 + 2304); } } } while (0)
#define WAITSET(n, XS, WL, WH) asm volatile("s_waitcnt lgkmcnt(" #n ")" : "+v"(XS[0]), "+v"(XS[1]), \
    "+v"(WL[0]), "+v"(WL[1]), "+v"(WL[2]), "+v"(WL[3]), "+v"(WL[4]), "+v"(WL[5]), "+v"(WL[6]), "+v"(WL[7]), "+v"(WL[8]), \
    "+v"(WH[0]), "+v"(WH[1]), "+v"(WH[2]), "+v"(WH[3]), "+v"(WH[4]), "+v"(WH[5]), "+v"(WH[6]), "+v"(WH[7]), "+v"(WH[8]))
#define MMASET(XS, WL, WH) do { _Pragma("unroll") for (int nt_ = 0; nt_ < 9; ++nt_) { s16x8 wb_ = cat8(WL[nt_], WH[nt_]); \
    acc[nt_][0] = __builtin_amdgcn_mfma_f32_16x16x32_bf16(wb_, XS[0], acc[nt_][0], 0, 0, 0); \
    acc[nt_][1] = __builtin_amdgcn_mfma_f32_16x16x32_bf16(wb_, XS[1], acc[nt_][1], 0, 0, 0); } } while (0)
template <int OFF> __device__ __forceinline__ s16x4 rd_tr_t(unsigned addr) { s16x4 r; asm volatile("ds_read_b64_tr_b16 %0, %1 offset:%2" : "=v"(r) : "v"(addr), "i"(OFF)); return r; }
#define RD_TRX(dst, addr, off) dst = rd_tr_sel(addr, off)
__device__ __forceinline__ s16x4 rd_tr_sel(unsigned addr, int off) {
  switch (off) {
#define C_(o) case o: return rd_tr_t<o>(addr);
    C_(0) C_(32) C_(64) C_(96) C_(128) C_(160) C_(192) C_(224) C_(256)
    C_(2304) C_(2336) C_(2368) C_(2400) C_(2432) C_(2464) C_(2496) C_(2528) C_(2560)
#undef C_
  }
  return rd_tr_t<0>(addr);
}

constexpr int LN_ROWS = 16;
constexpr int LN_TS = 592;

struct cplx { float re, im; };
__device__ __forceinline__ cplx cmul(cplx a, cplx b) { return {a.re * b.re - a.im * b.im, a.re * b.im + a.im * b.re}; }
__device__ __forceinline__ cplx csq(cplx a) { return {a.re * a.re - a.im * a.im, 2.f * a.re * a.im}; }
__device__ __forceinline__ cplx cexp_e(float arl2, float air, float e) {
  float m = __builtin_amdgcn_exp2f(arl2 * e);
  float th = air * e;
  th = th - floorf(th);
  float s = __builtin_amdgcn_sinf(th);
  float c = __builtin_amdgcn_cosf(th);
  return {m * c, m * s};
}

constexpr int LN_BLOCKS = L / LN_ROWS;
__global__ __launch_bounds__(512) void ln_kernel(const u16* __restrict__ x, u16* __restrict__ xn, u16* __restrict__ uT,
                                                 int* __restrict__ counters,
    const u16* __restrict__ Are, const u16* __restrict__ Aim,
    const u16* __restrict__ Bre, const u16* __restrict__ Bim, const u16* __restrict__ Cre,
    const u16* __restrict__ Cim, const u16* __restrict__ logdt, u16* __restrict__ KR, float* __restrict__ Ksc,
    const u16* __restrict__ w1, const u16* __restrict__ w2, unsigned* __restrict__ sink) {
  __shared__ __attribute__((aligned(16))) char smem[27648];
  const int tid = threadIdx.x, w = tid >> 6, l = tid & 63;
  if (blockIdx.x >= LN_BLOCKS + D) {
    const int p = blockIdx.x - (LN_BLOCKS + D), xq = (p & 7) >> 1, idx = p >> 3;
    u32x4 v1[6], v2[6];
    const u32x4* w2p = (const u32x4*)(w2 + (size_t)(1152 * xq + 36 * idx) * D);
#pragma unroll
    for (int j = 0; j < 6; ++j) {
      int i = tid + 512 * j;
      if (i >= 18 * 144) i = tid;
      const int r = i / 144, c = i - r * 144;
      v1[j] = *(const u32x4*)(w1 + (size_t)(18 * idx + r) * F + 1152 * xq + c * 8);
      v2[j] = w2p[i];
    }
    u32x4 accv = {0u, 0u, 0u, 0u};
#pragma unroll
    for (int j = 0; j < 6; ++j) accv ^= v1[j] ^ v2[j];
    const unsigned v = accv[0] ^ accv[1] ^ accv[2] ^ accv[3];
    if (v == 0x9e3779b9u) sink[tid] = v;
    return;
  }
  const int bx = blockIdx.x;
  if (bx >= LN_BLOCKS) {
    const int d = bx - LN_BLOCKS;
    float* red = (float*)(smem);
    float* Kd = (float*)(smem + 24576);
    {
      const int idx = l & 15, kk = l >> 4;
      const int nb = d * NS + 16 * w + 4 * kk;
      const u32x2 vAr = *(const u32x2*)(Are + nb), vAi = *(const u32x2*)(Aim + nb), vBr = *(const u32x2*)(Bre + nb),
                  vBi = *(const u32x2*)(Bim + nb), vCr = *(const u32x2*)(Cre + nb), vCi = *(const u32x2*)(Cim + nb);
      const float dt = rbf(__expf(bf2f(logdt[d])));
      const float e0 = (float)(128 * (idx >> 3) + (idx & 7));
      const float e1 = idx < 8 ? (float)(256 + 128 * (idx >> 2) + 2 * (idx & 3)) : (float)(512 + 128 * ((idx >> 1) & 3) + 4 * (idx & 1));
      const float e2 = (float)(1024 + 128 * idx);
      const float eb = (float)(8 * idx);
      float av[3][8], bv[8];
#pragma unroll
      for (int i = 0; i < 4; ++i) {
#define SEL_(v) ((i & 1) ? bfhi(v[i >> 1]) : bflo(v[i >> 1]))
        const float ar = dt * SEL_(vAr), ai = dt * SEL_(vAi), br = dt * SEL_(vBr), bi = dt * SEL_(vBi);
        const float cr = SEL_(vCr), ci = SEL_(vCi);
#undef SEL_
        const cplx cb = {cr * br - ci * bi, cr * bi + ci * br};
        const float arl2 = ar * 1.4426950408889634f;
        const float air = ai * 0.15915494309189535f;
        const cplx A0 = cexp_e(arl2, air, e0), A1 = cexp_e(arl2, air, e1), A2 = cexp_e(arl2, air, e2);
        const cplx B = cmul(cb, cexp_e(arl2, air, eb));
        av[0][2 * i] = A0.re; av[0][2 * i + 1] = A0.im;
        av[1][2 * i] = A1.re; av[1][2 * i + 1] = A1.im;
        av[2][2 * i] = A2.re; av[2][2 * i + 1] = A2.im;
        bv[2 * i] = B.re; bv[2 * i + 1] = -B.im;
      }
      u32x4 ah[3], al[3], bh, bl;
#pragma unroll
      for (int j = 0; j < 4; ++j) {
#pragma unroll
        for (int b = 0; b < 3; ++b) {
          const unsigned h = pack_bf16(av[b][2 * j], av[b][2 * j + 1]);
          ah[b][j] = h;
          al[b][j] = pack_bf16(av[b][2 * j] - bflo(h), av[b][2 * j + 1] - bfhi(h));
        }
        const unsigned h = pack_bf16(bv[2 * j], bv[2 * j + 1]);
        bh[j] = h;
        bl[j] = pack_bf16(bv[2 * j] - bflo(h), bv[2 * j + 1] - bfhi(h));
      }
#pragma unroll
      for (int b = 0; b < 3; ++b) {
        f32x4 ka = {0.f, 0.f, 0.f, 0.f};
        ka = __builtin_amdgcn_mfma_f32_16x16x32_bf16(__builtin_bit_cast(s16x8, al[b]), __builtin_bit_cast(s16x8, bh), ka, 0, 0, 0);
        ka = __builtin_amdgcn_mfma_f32_16x16x32_bf16(__builtin_bit_cast(s16x8, ah[b]), __builtin_bit_cast(s16x8, bl), ka, 0, 0, 0);
        ka = __builtin_amdgcn_mfma_f32_16x16x32_bf16(__builtin_bit_cast(s16x8, ah[b]), __builtin_bit_cast(s16x8, bh), ka, 0, 0, 0);
#pragma unroll
        for (int r = 0; r < 4; ++r) red[(w * 3 + b) * 256 + (4 * kk + r) * 16 + idx] = ka[r];
      }
    }
    __syncthreads();
    for (int e = tid; e < 768; e += 512) {
      float sacc = red[e];
#pragma unroll
      for (int ww = 1; ww < 8; ++ww) sacc += red[ww * 768 + e];
      Kd[e] = sacc;
    }
    __syncthreads();
    {
      float kv[4];
      float mx = 0.f;
#pragma unroll
      for (int h = 0; h < 4; ++h) {
        const int p = 4 * tid + h;
        const int ll = 2047 - p;
        const int lr = (int)rbf((float)ll);
        const int q = (lr >> 3) & 15;
        int row;
        if (lr < 256) row = ((lr >> 7) << 3) + (lr & 7);
        else if (lr < 512) row = 16 + (((lr >> 7) & 1) << 2) + ((lr & 7) >> 1);
        else if (lr < 1024) row = 24 + (((lr >> 7) & 3) << 1) + ((lr & 7) >> 2);
        else row = 32 + ((lr - 1024) >> 7);
        kv[h] = Kd[row * 16 + q];
        mx = fmaxf(mx, fabsf(kv[h]));
      }
#pragma unroll
      for (int o = 1; o < 64; o <<= 1) mx = fmaxf(mx, __shfl_xor(mx, o));
      float* wmx = red;
      if (l == 0) wmx[w] = mx;
      __syncthreads();
      mx = wmx[0];
#pragma unroll
      for (int ww = 1; ww < 8; ++ww) mx = fmaxf(mx, wmx[ww]);
      int ex = (int)((__float_as_uint(mx) >> 23) & 255u);
      int se = 137 - ex;
      se = se > 120 ? 120 : (se < -120 ? -120 : se);
      const float sc = __uint_as_float((unsigned)(127 + se) << 23);
      typedef __attribute__((ext_vector_type(2))) _Float16 h16x2;
      h16x2 p0 = {(_Float16)(kv[0] * sc), (_Float16)(kv[1] * sc)};
      h16x2 p1 = {(_Float16)(kv[2] * sc), (_Float16)(kv[3] * sc)};
      u32x2 o2 = {__builtin_bit_cast(unsigned, p0), __builtin_bit_cast(unsigned, p1)};
      *(u32x2*)(KR + (size_t)d * 2048 + 4 * tid) = o2;
      if (tid == 0) Ksc[d] = __uint_as_float((unsigned)(127 - se) << 23);
    }
    return;
  }
  if (bx < 4) counters[bx * 512 + tid] = 0;
  const int t0 = bx * LN_ROWS;
  const int r = w * 2 + (l >> 5), c = l & 31;
  const bool act_l = c < 24;
  const u32x4* xr = (const u32x4*)(x + (size_t)(t0 + r) * D);
  u32x4 v[3];
#pragma unroll
  for (int i = 0; i < 3; ++i) v[i] = act_l ? xr[c + 24 * i] : (u32x4){0u, 0u, 0u, 0u};
  float s = 0.f;
#pragma unroll
  for (int i = 0; i < 3; ++i)
#pragma unroll
    for (int j = 0; j < 4; ++j) s += bflo(v[i][j]) + bfhi(v[i][j]);
#pragma unroll
  for (int o = 1; o < 32; o <<= 1) s += __shfl_xor(s, o);
  const float mean = s * (1.0f / D);
  float q = 0.f;
#pragma unroll
  for (int i = 0; i < 3; ++i)
#pragma unroll
    for (int j = 0; j < 4; ++j) {
      float a = bflo(v[i][j]) - mean, b = bfhi(v[i][j]) - mean;
      q += a * a + b * b;
    }
  if (!act_l) q = 0.f;
#pragma unroll
  for (int o = 1; o < 32; o <<= 1) q += __shfl_xor(q, o);
  const float var = q * (1.0f / D);
  const float mb = rbf(mean), vb = rbf(var);
  const float den = rbf(sqrtf(rbf(vb + 1e-5f)));
  const float rc = 1.0f / den;
  u32x4* xo = (u32x4*)(xn + (size_t)(t0 + r) * D);
  if (act_l) {
#pragma unroll
    for (int i = 0; i < 3; ++i) {
      u32x4 o;
#pragma unroll
      for (int j = 0; j < 4; ++j) {
        const float da = rbf(bflo(v[i][j]) - mb), db = rbf(bfhi(v[i][j]) - mb);
        float qa = da * rc, qb = db * rc;
        qa = fmaf(fmaf(-qa, den, da), rc, qa);
        qb = fmaf(fmaf(-qb, den, db), rc, qb);
        o[j] = pack_bf16(qa, qb);
      }
      xo[c + 24 * i] = o;
      *(u32x4*)(smem + (r * LN_TS + (c + 24 * i) * 8) * 2) = o;
    }
  }
  __syncthreads();
  const unsigned sbase = (unsigned)(uintptr_t)smem;
  const int g = l >> 4, qq = (l & 15) >> 2, p = l & 3;
  for (int it = w; it < 18; it += 8) {
    const int d0 = it * 32 + (g >> 1) * 16;
    const int rb = (g & 1) * 8;
    s16x4 a = tr_read(sbase + ((rb + qq) * LN_TS + d0 + 4 * p) * 2);
    s16x4 b = tr_read(sbase + ((rb + 4 + qq) * LN_TS + d0 + 4 * p) * 2);
    *(s16x8*)(uT + (size_t)(d0 + (l & 15)) * L + t0 + rb) = cat8(a, b);
  }
}

constexpr int G1_A_BYTES = 128 * 32 * 2;
constexpr int G1_B_BYTES = 32 * 288 * 2;
constexpr int G1_STAGE = G1_A_BYTES + G1_B_BYTES;
constexpr int CV2_UC = 4160;
constexpr int CV2_K = 2 * CV2_UC;
constexpr int CV2_BYTES = CV2_K + 4672 + 32;
constexpr int G1_LDS = 4 * G1_STAGE + 3 * CV2_BYTES;

constexpr int CV_UC_STRIDE = 4160;
constexpr int CV_K_OFF = 4 * CV_UC_STRIDE;
constexpr int CV_K_BYTES = 2336 * 2;
constexpr int CV_KD_OFF = CV_K_OFF + 2 * CV_K_BYTES;
constexpr int CV_CH_STRIDE = 32768;
constexpr int CV_BLOCKS = D / 3;

__device__ __forceinline__ float mish_f(float h) {
  float t = fminf(h * 1.4426950408889634f, 43.f);
  float e = __builtin_amdgcn_exp2f(t);
  float a = e + 2.f;
  float n = e * a;
  float n2 = fmaf(e, a, 2.f);
  return h * (n * __builtin_amdgcn_rcpf(n2));
}

__device__ __forceinline__ unsigned mish2(float h0, float h1) {
  const f32x2 h = {h0, h1};
  f32x2 t = h * 1.4426950408889634f;
  f32x2 e = {__builtin_amdgcn_exp2f(fminf(t[0], 43.f)), __builtin_amdgcn_exp2f(fminf(t[1], 43.f))};
  f32x2 a = e + 2.f;
  f32x2 n = e * a;
  f32x2 n2 = n + 2.f;
  f32x2 r = {__builtin_amdgcn_rcpf(n2[0]), __builtin_amdgcn_rcpf(n2[1])};
  f32x2 o = h * (n * r);
  return pack_bf16(o[0], o[1]);
}

__global__ __launch_bounds__(768, 3) void gemm1_conv_kernel(
    const u16* __restrict__ xn, const u16* __restrict__ w1, const u16* __restrict__ b1, u16* __restrict__ act,
    const u16* __restrict__ uT, const u16* __restrict__ KR, const float* __restrict__ Ksc, u16* __restrict__ yT) {
  extern __shared__ __attribute__((aligned(16))) char smem[];
  const unsigned sbase = (unsigned)(uintptr_t)smem;
  const int tid = threadIdx.x, l = tid & 63;
  const int w = __builtin_amdgcn_readfirstlane(tid >> 6);
  const int g = l >> 4, li = l & 15;
  if (blockIdx.x < 256) {
    const int b = blockIdx.x, xcd = b & 7, idx = b >> 3;
    const int mi = 8 * (xcd & 1) + (idx & 7), ni = 4 * (xcd >> 1) + (idx >> 3);
    const int m0 = mi * 128, n0 = ni * 288;
    if (w >= 8) {
      const int lw = w - 8;
      int aoff[2], boff[5];
#pragma unroll
      for (int j = 0; j < 2; ++j) {
        const int p = (lw + 4 * j) * 64 + l;
        const int row = p >> 2, chp = p & 3;
        const int f = (4 - ((row >> 2) & 3)) & 3;
        aoff[j] = (m0 + row) * D + ((chp ^ f) * 8);
      }
#pragma unroll
      for (int j = 0; j < 5; ++j) {
        int ins = lw + 4 * j;
        if (ins >= 18) ins = 0;
        const int p = ins * 64 + l;
        const int row = p / 36, chp = p - row * 36;
        boff[j] = row * F + n0 + ((chp ^ (((row >> 3) & 1) << 1)) * 8);
      }
      auto stage = [&](int t, int buf) {
        const unsigned sb = sbase + buf * G1_STAGE;
#pragma unroll
        for (int j = 0; j < 2; ++j) glds16(xn + aoff[j] + t * 32, sb + (lw + 4 * j) * 1024);
#pragma unroll
        for (int j = 0; j < 4; ++j) glds16(w1 + boff[j] + (size_t)t * 32 * F, sb + G1_A_BYTES + (lw + 4 * j) * 1024);
        if (lw < 2) glds16(w1 + boff[4] + (size_t)t * 32 * F, sb + G1_A_BYTES + (lw + 16) * 1024);
      };
#define LVMWAIT(big, small) do { if (lw < 2) asm volatile("s_waitcnt vmcnt(" #big ")" ::: "memory"); else asm volatile("s_waitcnt vmcnt(" #small ")" ::: "memory"); } while (0)
      typedef __attribute__((ext_vector_type(8))) _Float16 h16x8;
      typedef __attribute__((ext_vector_type(2))) _Float16 h16x2;
      __builtin_amdgcn_s_setprio(1);
      const int d = b + 256 * lw;
      const bool hasc = d < D;
      const int coff = 4 * G1_STAGE + lw * CV2_BYTES;
      char* cs = smem + coff;
      const unsigned cbase = sbase + coff;
      float ysc = 0.f;
      {
        u32x4 cu[4], ck[4];
        unsigned cun[4];
        if (hasc) {
#pragma unroll
          for (int i = 0; i < 4; ++i) {
            const int e = 8 * (l + 64 * i);
            cu[i] = *(const u32x4*)(uT + (size_t)d * L + e);
            cun[i] = (e + 8 < L) ? *(const unsigned*)(uT + (size_t)d * L + e + 8) : 0u;
            ck[i] = *(const u32x4*)(KR + (size_t)d * 2048 + e);
          }
          ysc = Ksc[d];
        }
        stage(0, 0);
        stage(1, 1);
        stage(2, 2);
        if (hasc) {
          LVMWAIT(21, 18);
          asm volatile("" : "+v"(cu[0]), "+v"(cu[1]), "+v"(cu[2]), "+v"(cu[3]), "+v"(ck[0]), "+v"(ck[1]), "+v"(ck[2]), "+v"(ck[3]),
                       "+v"(cun[0]), "+v"(cun[1]), "+v"(cun[2]), "+v"(cun[3]), "+v"(ysc));
#pragma unroll
          for (int i = 0; i < 4; ++i) {
            const int tc = l + 64 * i;
            *(u32x4*)(cs + CV2_K + tc * 16) = ck[i];
            unsigned wv[5];
#pragma unroll
            for (int j = 0; j < 5; ++j) {
              const unsigned src = j < 4 ? cu[i][j] : cun[i];
              h16x2 hv = {(_Float16)bflo(src), (_Float16)bfhi(src)};
              wv[j] = __builtin_bit_cast(unsigned, hv);
            }
            u32x4 c0 = {wv[0], wv[1], wv[2], wv[3]}, c1;
#pragma unroll
            for (int j = 0; j < 4; ++j) c1[j] = __builtin_amdgcn_alignbyte(wv[j + 1], wv[j], 2);
            char* dst = cs + (8 * tc + 16) * 2;
            *(u32x4*)(dst) = c0;
            *(u32x4*)(dst + CV2_UC) = c1;
            if (i == 0) {
              const unsigned h0 = __shfl(wv[0], 0);
              if (l < 32) {
                const int c = l >> 4, ii = l & 15;
                *(u16*)(cs + c * CV2_UC + ii * 2) = (u16)((c == 1 && ii == 15) ? (h0 & 0xffffu) : 0u);
              }
            }
          }
          if (l < 36) *(u32x4*)(cs + CV2_K + 4096 + l * 16) = (u32x4){0u, 0u, 0u, 0u};
        }
      }
      f32x4 cacc[8];
#pragma unroll
      for (int j = 0; j < 8; ++j) cacc[j] = (f32x4){0.f, 0.f, 0.f, 0.f};
      const int cc = (li + 1) & 1;
      const unsigned ua = cbase + cc * CV2_UC + (li + 1 - cc + 8 * g) * 2;
      const unsigned kb = cbase + CV2_K + (2032 - 16 * li + 8 * g) * 2;
      LVMWAIT(14, 12);
      __builtin_amdgcn_s_barrier();
      for (int rho = 0; rho < 8; ++rho) {
        const int t = 2 * rho;
        u32x4 A[8];
        s16x8 Bh[8];
        LVMWAIT(7, 6);
        __builtin_amdgcn_s_barrier();
        stage(t + 3, (t + 3) & 3);
        if (hasc) {
#pragma unroll
          for (int s = 0; s < 8; ++s) {
            const unsigned ad = ua + (32 * (8 * s + rho)) * 2;
#pragma unroll
            for (int j = 0; j < 4; ++j) A[s][j] = *(LDS_AS unsigned*)(uintptr_t)(ad + 4 * j);
          }
#pragma unroll
          for (int dl = 0; dl < 8; ++dl) Bh[dl] = lds_read128(kb + (32 * rho - 256 * dl) * 2);
#pragma unroll
          for (int j = 0; j < 6; ++j)
#pragma unroll
            for (int s = 0; s <= j; ++s)
              cacc[j] = __builtin_amdgcn_mfma_f32_16x16x32_f16(__builtin_bit_cast(h16x8, A[s]), __builtin_bit_cast(h16x8, Bh[j - s]), cacc[j], 0, 0, 0);
        }
        LVMWAIT(7, 6);
        __builtin_amdgcn_s_barrier();
        if (t + 4 < 18) stage(t + 4, (t + 4) & 3);
        if (hasc) {
#pragma unroll
          for (int j = 6; j < 8; ++j)
#pragma unroll
            for (int s = 0; s <= j; ++s)
              cacc[j] = __builtin_amdgcn_mfma_f32_16x16x32_f16(__builtin_bit_cast(h16x8, A[s]), __builtin_bit_cast(h16x8, Bh[j - s]), cacc[j], 0, 0, 0);
        }
      }
      asm volatile("s_waitcnt vmcnt(0)" ::: "memory");
      __builtin_amdgcn_s_barrier();
      if (hasc) {
#pragma unroll
        for (int j = 0; j < 8; ++j) {
          const f32x4 sv = cacc[j] * ysc;
          u32x2 o = {pack_bf16(sv[0], sv[1]), pack_bf16(sv[2], sv[3])};
          *(u32x2*)(yT + (size_t)d * L + 256 * j + 16 * li + 4 * g) = o;
        }
      }
    } else {
      const int wm = w & 3, wn = w >> 2;
      const int fa = (4 - ((li >> 2) & 3)) & 3;
      const unsigned a_rd = (32 * wm + li) * 64 + ((g ^ fa) * 16);
      const int q = li >> 2, p4 = li & 3;
      const unsigned b_rd = G1_A_BYTES + (8 * g + q) * 576 + (p4 & 1) * 8;
      const int bsw = (g & 1) << 1;
      f32x4 acc[9][2];
#pragma unroll
      for (int nt = 0; nt < 9; ++nt) {
        const u32x2 bb = *(const u32x2*)(b1 + n0 + wn * 144 + nt * 16 + 4 * g);
        const f32x4 bv = {bflo(bb[0]), bfhi(bb[0]), bflo(bb[1]), bfhi(bb[1])};
#pragma unroll
        for (int mt = 0; mt < 2; ++mt) acc[nt][mt] = bv;
      }
      const unsigned bE0 = b_rd + (p4 >> 1) * 16 + wn * 288 + (wn ? -bsw * 16 : bsw * 16);
      const unsigned bO0 = b_rd + (p4 >> 1) * 16 + wn * 288 + (wn ? bsw * 16 : -bsw * 16);
      s16x8 xa0[2], xa1[2];
      s16x4 wl0[9], wh0[9], wl1[9], wh1[9];
      __builtin_amdgcn_s_barrier();
      {
        const unsigned pA = sbase + a_rd, pE = sbase + bE0, pO = sbase + bO0;
        RDSET(xa0, wl0, wh0, pA, 1024, pE, pO);
      }
      for (int t = 0; t < 16; t += 2) {
        {
          __builtin_amdgcn_s_barrier();
          const unsigned sbn = sbase + ((t + 1) & 3) * G1_STAGE;
          const unsigned pA = sbn + a_rd, pE = sbn + bE0, pO = sbn + bO0;
          WAITSET(0, xa0, wl0, wh0);
          RDSET(xa1, wl1, wh1, pA, 1024, pE, pO);
          MMASET(xa0, wl0, wh0);
        }
        {
          __builtin_amdgcn_s_barrier();
          const unsigned sbn = sbase + ((t + 2) & 3) * G1_STAGE;
          const unsigned pA = sbn + a_rd, pE = sbn + bE0, pO = sbn + bO0;
          WAITSET(0, xa1, wl1, wh1);
          RDSET(xa0, wl0, wh0, pA, 1024, pE, pO);
          MMASET(xa1, wl1, wh1);
        }
      }
      {
        __builtin_amdgcn_s_barrier();
        const unsigned sbn = sbase + (17 & 3) * G1_STAGE;
        const unsigned pA = sbn + a_rd, pE = sbn + bE0, pO = sbn + bO0;
        WAITSET(0, xa0, wl0, wh0);
        RDSET(xa1, wl1, wh1, pA, 1024, pE, pO);
        MMASET(xa0, wl0, wh0);
        WAITSET(0, xa1, wl1, wh1);
        MMASET(xa1, wl1, wh1);
      }
      int tz = threadIdx.x;
      asm volatile("" : "+v"(tz));
      const int li2 = tz & 15, g2 = (tz >> 4) & 3;
#pragma unroll
      for (int mt = 0; mt < 2; ++mt) {
        const int row = 32 * wm + 16 * mt + li2;
        const int sw = (row >> 1) & 7;
#pragma unroll
        for (int nt = 0; nt < 9; ++nt) {
          const int n = n0 + wn * 144 + nt * 16 + 4 * g2;
          const int kt = n >> 6, ch = (n >> 3) & 7;
          u16* dst = act + ((size_t)(mi * 72 + kt) * 128 + row) * 64 + ((ch ^ sw) * 8) + (n & 7);
          u32x2 o;
          o[0] = mish2(acc[nt][mt][0], acc[nt][mt][1]);
          o[1] = mish2(acc[nt][mt][2], acc[nt][mt][3]);
          *(u32x2*)(dst) = o;
          if (nt % 3 == 2) __builtin_amdgcn_sched_barrier(0);
        }
      }
    }
  }
}

constexpr int G2_A_BYTES = 128 * 64 * 2;
constexpr int G2_B_BYTES = 32 * 576;
constexpr int G2_STAGE = G2_A_BYTES + G2_B_BYTES;
constexpr int G2_NST = 4;
constexpr int G2_LDS = G2_NST * G2_STAGE;

__global__ __launch_bounds__(768, 3) void gemm2_kernel(
    const u16* __restrict__ act, const u16* __restrict__ w2, float* __restrict__ slabs, int* __restrict__ counters,
    const u16* __restrict__ x, const u16* __restrict__ xn, const u16* __restrict__ yT, const u16* __restrict__ Dskip,
    u16* __restrict__ out) {
  extern __shared__ __attribute__((aligned(16))) char smem[];
  STAMP(0);
  const unsigned sbase = (unsigned)(uintptr_t)smem;
  const int tid = threadIdx.x, l = tid & 63;
  const int w = __builtin_amdgcn_readfirstlane(tid >> 6);
  const int g = l >> 4, li = l & 15;
  const int b = blockIdx.x, xcd = b & 7, idx = b >> 3;
  const int mi = 8 * (xcd & 1) + (idx & 7), ks = xcd >> 1, ni = idx >> 3;
  const int m0 = mi * 128, n0 = ni * 144, k0 = ks * 1152;
  const int wm = w & 3, wk = (w >> 2) & 1;
  f32x4 acc[9][2];
#pragma unroll
  for (int nt = 0; nt < 9; ++nt)
#pragma unroll
    for (int mt = 0; mt < 2; ++mt) acc[nt][mt] = (f32x4){0.f, 0.f, 0.f, 0.f};
  const int R0 = m0 + 32 * ks;
  const int uh = w >= 5 ? 1 : 0, uj = w - 5 * uh;
  const int ntile = (w >= 10) ? 0 : (uj < 4 ? 2 : 1);
  u32x2 xv[2], nv[2], dv[2];
  unsigned yv[2][4];
  auto load_ops = [&]() {
#pragma unroll
    for (int i = 0; i < 2; ++i) {
      int nt = 2 * uj + i;
      nt = nt > 8 ? 8 : nt;
      const int row = R0 + 16 * uh + li, col = n0 + nt * 16 + 4 * g;
      const size_t go = (size_t)row * D + col;
      xv[i] = *(const u32x2*)(x + go);
      nv[i] = *(const u32x2*)(xn + go);
      dv[i] = *(const u32x2*)(Dskip + col);
#pragma unroll
      for (int e = 0; e < 4; ++e) yv[i][e] = yT[(size_t)(col + e) * L + row];
    }
  };
  if (w >= 8) {
    const int lw = w - 8;
    const u16* aptr = act + (size_t)(mi * 72 + ks * 18) * 8192 + (lw * 64 + l) * 8;
    int boff[5];
#pragma unroll
    for (int j = 0; j < 5; ++j) {
      int ins = lw + 4 * j;
      if (ins >= 18) ins = 0;
      const int p = ins * 64 + l;
      const int row = p / 36, chp = p - row * 36;
      const int ch = chp ^ (((row >> 3) & 1) << 1);
      const int half = ch >= 18 ? 1 : 0;
      boff[j] = (k0 + row + 32 * half) * D + n0 + (ch - 18 * half) * 8;
    }
    auto stage = [&](int t, int buf) {
      const unsigned sb = sbase + buf * G2_STAGE;
#pragma unroll
      for (int j = 0; j < 4; ++j) glds16(aptr + t * 8192 + j * 2048, sb + (lw + 4 * j) * 1024);
#pragma unroll
      for (int j = 0; j < 4; ++j) glds16(w2 + boff[j] + t * 64 * D, sb + G2_A_BYTES + (lw + 4 * j) * 1024);
      if (lw < 2) glds16(w2 + boff[4] + t * 64 * D, sb + G2_A_BYTES + (lw + 16) * 1024);
    };
    stage(0, 0);
    stage(1, 1);
    stage(2, 2);
    LVMWAIT(18, 16);
    __builtin_amdgcn_s_barrier();
    for (int t = 0; t < 15; ++t) {
      LVMWAIT(9, 8);
      __builtin_amdgcn_s_barrier();
      stage(t + 3, (t + 3) & 3);
    }
    asm volatile("" ::: "memory");
    load_ops();
    asm volatile("" ::: "memory");
    LVMWAIT(23, 22);
    __builtin_amdgcn_s_barrier();
    asm volatile("s_waitcnt vmcnt(14)" ::: "memory");
    __builtin_amdgcn_s_barrier();
  } else {
    const unsigned a_rd = (32 * wm + li) * 128 + (((4 * wk + g) ^ ((li >> 1) & 7)) * 16);
    const int q = li >> 2, p4 = li & 3;
    const unsigned b_rd = G2_A_BYTES + (8 * g + q) * 576 + (p4 & 1) * 8;
    const int bsw = (g & 1) << 1;
    const unsigned bE0 = b_rd + (p4 >> 1) * 16 + wk * 288 + (wk ? -bsw * 16 : bsw * 16);
    const unsigned bO0 = b_rd + (p4 >> 1) * 16 + wk * 288 + (wk ? bsw * 16 : -bsw * 16);
    s16x8 xa0[2], xa1[2];
    s16x4 wl0[9], wh0[9], wl1[9], wh1[9];
    __builtin_amdgcn_s_barrier();
    STAMP(1);
    {
      const unsigned pA = sbase + a_rd, pE = sbase + bE0, pO = sbase + bO0;
      RDSET(xa0, wl0, wh0, pA, 2048, pE, pO);
    }
    asm volatile("" ::: "memory");
    load_ops();
    asm volatile("" ::: "memory");
    for (int t = 0; t < 16; t += 2) {
      {
        __builtin_amdgcn_s_barrier();
        const unsigned sbn = sbase + ((t + 1) & 3) * G2_STAGE;
        const unsigned pA = sbn + a_rd, pE = sbn + bE0, pO = sbn + bO0;
        WAITSET(0, xa0, wl0, wh0);
        RDSET(xa1, wl1, wh1, pA, 2048, pE, pO);
        MMASET(xa0, wl0, wh0);
      }
      {
        __builtin_amdgcn_s_barrier();
        const unsigned sbn = sbase + ((t + 2) & 3) * G2_STAGE;
        const unsigned pA = sbn + a_rd, pE = sbn + bE0, pO = sbn + bO0;
        WAITSET(0, xa1, wl1, wh1);
        RDSET(xa0, wl0, wh0, pA, 2048, pE, pO);
        MMASET(xa1, wl1, wh1);
      }
    }
    {
      __builtin_amdgcn_s_barrier();
      const unsigned sbn = sbase + (17 & 3) * G2_STAGE;
      const unsigned pA = sbn + a_rd, pE = sbn + bE0, pO = sbn + bO0;
      WAITSET(0, xa0, wl0, wh0);
      RDSET(xa1, wl1, wh1, pA, 2048, pE, pO);
      MMASET(xa0, wl0, wh0);
      WAITSET(0, xa1, wl1, wh1);
      MMASET(xa1, wl1, wh1);
    }
  }
  __syncthreads();
  STAMP(2);
  f32x4 res[9];
  {
    f32x4* ex = (f32x4*)smem;
    if (w < 4) {
#pragma unroll
      for (int nt = 0; nt < 9; ++nt) ex[(w * 9 + nt) * 64 + l] = acc[nt][1];
    } else if (w < 8) {
#pragma unroll
      for (int nt = 0; nt < 9; ++nt) ex[(w * 9 + nt) * 64 + l] = acc[nt][0];
    }
    __syncthreads();
    const int pw = (w ^ 4) & 7;
    if (w < 4) {
#pragma unroll
      for (int nt = 0; nt < 9; ++nt) res[nt] = acc[nt][0] + ex[(pw * 9 + nt) * 64 + l];
    } else {
#pragma unroll
      for (int nt = 0; nt < 9; ++nt) res[nt] = acc[nt][1] + ex[(pw * 9 + nt) * 64 + l];
    }
  }
  const int grp = mi * 4 + ni;
  {
    float* gslab = slabs + (size_t)grp * 4 * 4 * 2 * 9 * 64 * 4;
    auto rs = __builtin_amdgcn_make_buffer_rsrc((void*)gslab, 0, 4 * 4 * 2 * 9 * 64 * 16, 0x00020000);
    __syncthreads();
    f32x4* own = (f32x4*)smem;
    int* flags = counters + grp * 32;
    if (w >= 8) {
    } else if (wm == ks) {
#pragma unroll
      for (int nt = 0; nt < 9; ++nt) own[(wk * 9 + nt) * 64 + l] = res[nt];
    } else {
      const int base = ((wm * 4 + ks) * 2 + wk) * 4608;
#pragma unroll
      for (int j = 0; j < 4; ++j) {
        const u32x4 pk = {pack_bf16(res[2 * j][0], res[2 * j][1]), pack_bf16(res[2 * j][2], res[2 * j][3]),
                          pack_bf16(res[2 * j + 1][0], res[2 * j + 1][1]), pack_bf16(res[2 * j + 1][2], res[2 * j + 1][3])};
        __builtin_amdgcn_raw_buffer_store_b128(pk, rs, base + j * 1024 + l * 16, 0, 16);
      }
      const u32x2 pk8 = {pack_bf16(res[8][0], res[8][1]), pack_bf16(res[8][2], res[8][3])};
      __builtin_amdgcn_raw_buffer_store_b64(pk8, rs, base + 4096 + l * 8, 0, 16);
      asm volatile("s_waitcnt vmcnt(0)" ::: "memory");
      if (l == 0) __hip_atomic_store(&flags[(wm * 4 + ks) * 2 + wk], 1, __ATOMIC_RELAXED, __HIP_MEMORY_SCOPE_AGENT);
    }
    STAMP(3);
    asm volatile("s_waitcnt lgkmcnt(0)" ::: "memory");
    __builtin_amdgcn_s_barrier();
    if (ntile > 0) {
      {
        const int src = l & 3;
        const bool need = (l < 4) && (src != ks);
        for (int spin = 0; spin < (1 << 20); ++spin) {
          int v = 1;
          if (need) v = __hip_atomic_load(&flags[(ks * 4 + src) * 2 + uh], __ATOMIC_RELAXED, __HIP_MEMORY_SCOPE_AGENT);
          if (__all(v != 0)) break;
          __builtin_amdgcn_s_sleep(1);
        }
        asm volatile("" ::: "memory");
      }
      STAMP(4);
      u32x4 pv[3];
#pragma unroll
      for (int k = 0; k < 3; ++k) {
        const int src = k + (k >= ks ? 1 : 0);
        const int base = ((ks * 4 + src) * 2 + uh) * 4608;
        if (uj < 4) pv[k] = __builtin_amdgcn_raw_buffer_load_b128(rs, base + uj * 1024 + l * 16, 0, 16);
        else { const u32x2 t2 = __builtin_amdgcn_raw_buffer_load_b64(rs, base + 4096 + l * 8, 0, 16); pv[k] = (u32x4){t2[0], t2[1], 0u, 0u}; }
      }
#pragma unroll
      for (int i = 0; i < 2; ++i) {
        if (i < ntile) {
          const int nt = 2 * uj + i;
          const int row = R0 + 16 * uh + li, col = n0 + nt * 16 + 4 * g;
          f32x4 parts[4];
          const f32x4 mine = own[(uh * 9 + nt) * 64 + l];
#pragma unroll
          for (int k = 0; k < 4; ++k) {
            const int kk = k - (k > ks ? 1 : 0);
            const u32x4 pq = pv[kk < 3 ? kk : 2];
            const f32x4 rem = {bflo(pq[2 * i]), bfhi(pq[2 * i]), bflo(pq[2 * i + 1]), bfhi(pq[2 * i + 1])};
            parts[k] = (k == ks) ? mine : rem;
          }
          const f32x4 sum = (parts[0] + parts[1]) + (parts[2] + parts[3]);
          float o[4];
#pragma unroll
          for (int e = 0; e < 4; ++e) {
            const float xf = (e & 1) ? bfhi(xv[i][e >> 1]) : bflo(xv[i][e >> 1]);
            const float nf = (e & 1) ? bfhi(nv[i][e >> 1]) : bflo(nv[i][e >> 1]);
            const float df = (e & 1) ? bfhi(dv[i][e >> 1]) : bflo(dv[i][e >> 1]);
            const float ya = rbf(bflo(yv[i][e]) + rbf(nf * df));
            o[e] = rbf(xf + ya) + rbf(sum[e]);
          }
          u32x2 ov = {pack_bf16(o[0], o[1]), pack_bf16(o[2], o[3])};
          *(u32x2*)(out + (size_t)row * D + col) = ov;
        }
      }
    }
  }
}

extern "C" void kernel_launch(void* const* d_in, const int* in_sizes, int n_in,
                              void* d_out, int out_size, void* d_ws, size_t ws_size,
                              hipStream_t stream) {
  const u16* x = (const u16*)d_in[0];
  const u16* Are = (const u16*)d_in[1];
  const u16* Aim = (const u16*)d_in[2];
  const u16* Bre = (const u16*)d_in[3];
  const u16* Bim = (const u16*)d_in[4];
  const u16* Cre = (const u16*)d_in[5];
  const u16* Cim = (const u16*)d_in[6];
  const u16* logdt = (const u16*)d_in[7];
  const u16* Dskip = (const u16*)d_in[8];
  const u16* w1 = (const u16*)d_in[10];
  const u16* b1 = (const u16*)d_in[11];
  const u16* w2 = (const u16*)d_in[12];
  u16* out = (u16*)d_out;
  char* ws = (char*)d_ws;
  u16* xn = (u16*)(ws);
  u16* uT = (u16*)(ws + (4 << 20));
  u16* yT = (u16*)(ws + (8 << 20));
  u16* KR = (u16*)(ws + (12 << 20));
  int* counters = (int*)(ws + (20 << 20));
  float* Ksc = (float*)(ws + (21 << 20));
  u16* act = (u16*)(ws + (24 << 20));
  float* slabs = (float*)(ws + (48 << 20));
  hipFuncSetAttribute((const void*)gemm1_conv_kernel, hipFuncAttributeMaxDynamicSharedMemorySize, G1_LDS);
  hipFuncSetAttribute((const void*)gemm2_kernel, hipFuncAttributeMaxDynamicSharedMemorySize, G2_LDS);
  ln_kernel<<<LN_BLOCKS + D + 256, 512, 0, stream>>>(x, xn, uT, counters, Are, Aim, Bre, Bim, Cre, Cim, logdt, KR, Ksc, w1, w2, (unsigned*)(ws + (22 << 20)));
  gemm1_conv_kernel<<<256, 768, G1_LDS, stream>>>(xn, w1, b1, act, uT, KR, Ksc, yT);
  gemm2_kernel<<<256, 768, G2_LDS, stream>>>(act, w2, slabs, counters, x, xn, yT, Dskip, out);
}
```

```cpp
#include <hip/hip_runtime.h>
#include <hip/hip_bf16.h>
#include <stdint.h>

typedef unsigned short u16;
typedef __attribute__((ext_vector_type(4))) short s16x4;
typedef __attribute__((ext_vector_type(8))) short s16x8;
typedef __attribute__((ext_vector_type(4))) float f32x4;
typedef __attribute__((ext_vector_type(2))) float f32x2;
typedef __attribute__((ext_vector_type(4))) unsigned u32x4;
typedef __attribute__((ext_vector_type(2))) unsigned u32x2;
typedef __attribute__((ext_vector_type(2))) __bf16 bf16x2_t;
#define LDS_AS __attribute__((address_space(3)))

constexpr int L = 2048, D = 576, NS = 128, F = 4608;
#define STAMP(i)

__device__ __forceinline__ float bf2f(u16 v) { return __uint_as_float(((unsigned)v) << 16); }
__device__ __forceinline__ float bflo(unsigned v) { return __uint_as_float(v << 16); }
__device__ __forceinline__ float bfhi(unsigned v) { return __uint_as_float(v & 0xffff0000u); }
__device__ __forceinline__ unsigned pack_bf16(float a, float b) {
  f32x2 v = {a, b};
  bf16x2_t r = __builtin_convertvector(v, bf16x2_t);
  return __builtin_bit_cast(unsigned, r);
}
__device__ __forceinline__ float rbf(float f) { return __uint_as_float(pack_bf16(f, 0.f) << 16); }

__device__ __forceinline__ void glds16(const void* g, unsigned lds_byte_addr_uniform) {
  __builtin_amdgcn_global_load_lds((const unsigned*)g, (LDS_AS unsigned*)(uintptr_t)lds_byte_addr_uniform, 16, 0, 0);
}
__device__ __forceinline__ s16x4 tr_read(unsigned lds_addr) {
  return __builtin_amdgcn_ds_read_tr16_b64_v4i16((LDS_AS s16x4*)(uintptr_t)lds_addr);
}
__device__ __forceinline__ s16x8 lds_read128(unsigned lds_addr) {
  return *(LDS_AS s16x8*)(uintptr_t)lds_addr;
}
__device__ __forceinline__ s16x4 lds_read64(unsigned lds_addr) {
  return *(LDS_AS s16x4*)(uintptr_t)lds_addr;
}
__device__ __forceinline__ s16x8 cat8(s16x4 a, s16x4 b) { return __builtin_shufflevector(a, b, 0, 1, 2, 3, 4, 5, 6, 7); }

#define RD_TR(dst, addr, off) asm volatile("ds_read_b64_tr_b16 %0, %1 offset:%2" : "=v"(dst) : "v"(addr), "i"(off))
#define RD128(dst, addr, off) asm volatile("ds_read_b128 %0, %1 offset:%2" : "=v"(dst) : "v"(addr), "i"(off))
#define WAIT_WB(n, s) asm volatile("s_waitcnt lgkmcnt(" #n ")" : "+v"(wlo[s]), "+v"(whi[s]))
#define WAIT_WB_XA(n, s, XA) asm volatile("s_waitcnt lgkmcnt(" #n ")" : "+v"(wlo[s]), "+v"(whi[s]), "+v"(XA[0]), "+v"(XA[1]))
#define ISSUE_WB(nt, AE, AO) do { if ((nt) & 1) { RD_TR(wlo[(nt) % 3], AO, (nt) * 32); RD_TR(whi[(nt) % 3], AO, (nt) * 32 + 2304); } \
                                  else { RD_TR(wlo[(nt) % 3], AE, (nt) * 32); RD_TR(whi[(nt) % 3], AE, (nt) * 32 + 2304); } } while (0)
#define MMA4(nt, XA) do { s16x8 wb_ = cat8(wlo[(nt) % 3], whi[(nt) % 3]); \
    _Pragma("unroll") for (int mt = 0; mt < 2; ++mt) acc[nt][mt] = __builtin_amdgcn_mfma_f32_16x16x32_bf16(wb_, XA[mt], acc[nt][mt], 0, 0, 0); } while (0)
#define GSTAGE(XC, XN, AMT, SYNC_CODE, HAVE_NEXT) do { \
    ISSUE_WB(2, cE, cO); WAIT_WB_XA(4, 0, XC); MMA4(0, XC); \
    ISSUE_WB(3, cE, cO); WAIT_WB(4, 1); MMA4(1, XC); \
    ISSUE_WB(4, cE, cO); WAIT_WB(4, 2); MMA4(2, XC); \
    ISSUE_WB(5, cE, cO); WAIT_WB(4, 0); MMA4(3, XC); \
    ISSUE_WB(6, cE, cO); WAIT_WB(4, 1); MMA4(4, XC); \
    ISSUE_WB(7, cE, cO); WAIT_WB(4, 2); MMA4(5, XC); \
    ISSUE_WB(8, cE, cO); WAIT_WB(4, 0); MMA4(6, XC); \
    asm volatile("s_waitcnt lgkmcnt(0)" : "+v"(wlo[1]), "+v"(whi[1]), "+v"(wlo[2]), "+v"(whi[2])); \
    if (HAVE_NEXT) { \
      SYNC_CODE; \
      RD128(XN[0], nA, 0); RD128(XN[1], nA, AMT); \
      ISSUE_WB(0, nE, nO); \
    } \
    MMA4(7, XC); \
    if (HAVE_NEXT) ISSUE_WB(1, nE, nO); \
    MMA4(8, XC); \
  } while (0)
#define VMWAIT2(big, small) do { if (w < 2) asm volatile("s_waitcnt vmcnt(" #big ")" ::: "memory"); else asm volatile("s_waitcnt vmcnt(" #small ")" ::: "memory"); } while (0)

#define RDSET(XS, WL, WH, pA, AMT, pE, pO) do { \
    RD128(XS[0], pA, 0); RD128(XS[1], pA, AMT); \
    _Pragma("unroll") for (int nt_ = 0; nt_ < 9; nt_ += 1) { \
      if (nt_ & 1) { RD_TRX(WL[nt_], pO, nt_ * 32); RD_TRX(WH[nt_], pO, nt_ * 32 + 2304); } \
      else { RD_TRX(WL[nt_], pE, nt_ * 32); RD_TRX(WH[nt_], pE, nt_ * 32 + 2304); } } } while (0)
#define WAITSET(n, XS, WL, WH) asm volatile("s_waitcnt lgkmcnt(" #n ")" : "+v"(XS[0]), "+v"(XS[1]), \
    "+v"(WL[0]), "+v"(WL[1]), "+v"(WL[2]), "+v"(WL[3]), "+v"(WL[4]), "+v"(WL[5]), "+v"(WL[6]), "+v"(WL[7]), "+v"(WL[8]), \
    "+v"(WH[0]), "+v"(WH[1]), "+v"(WH[2]), "+v"(WH[3]), "+v"(WH[4]), "+v"(WH[5]), "+v"(WH[6]), "+v"(WH[7]), "+v"(WH[8]))
#define MMASET(XS, WL, WH) do { _Pragma("unroll") for (int nt_ = 0; nt_ < 9; ++nt_) { s16x8 wb_ = cat8(WL[nt_], WH[nt_]); \
    acc[nt_][0] = __builtin_amdgcn_mfma_f32_16x16x32_bf16(wb_, XS[0], acc[nt_][0], 0, 0, 0); \
    acc[nt_][1] = __builtin_amdgcn_mfma_f32_16x16x32_bf16(wb_, XS[1], acc[nt_][1], 0, 0, 0); } } while (0)
template <int OFF> __device__ __forceinline__ s16x4 rd_tr_t(unsigned addr) { s16x4 r; asm volatile("ds_read_b64_tr_b16 %0, %1 offset:%2" : "=v"(r) : "v"(addr), "i"(OFF)); return r; }
#define RD_TRX(dst, addr, off) dst = rd_tr_sel(addr, off)
__device__ __forceinline__ s16x4 rd_tr_sel(unsigned addr, int off) {
  switch (off) {
#define C_(o) case o: return rd_tr_t<o>(addr);
    C_(0) C_(32) C_(64) C_(96) C_(128) C_(160) C_(192) C_(224) C_(256)
    C_(2304) C_(2336) C_(2368) C_(2400) C_(2432) C_(2464) C_(2496) C_(2528) C_(2560)
#undef C_
  }
  return rd_tr_t<0>(addr);
}

constexpr int LN_ROWS = 16;
constexpr int LN_TS = 592;

struct cplx { float re, im; };
__device__ __forceinline__ cplx cmul(cplx a, cplx b) { return {a.re * b.re - a.im * b.im, a.re * b.im + a.im * b.re}; }
__device__ __forceinline__ cplx csq(cplx a) { return {a.re * a.re - a.im * a.im, 2.f * a.re * a.im}; }
__device__ __forceinline__ cplx cexp_e(float arl2, float air, float e) {
  float m = __builtin_amdgcn_exp2f(arl2 * e);
  float th = air * e;
  th = __builtin_amdgcn_fractf(th);
  float s = __builtin_amdgcn_sinf(th);
  float c = __builtin_amdgcn_cosf(th);
  return {m * c, m * s};
}

constexpr int LN_BLOCKS = L / LN_ROWS;
__global__ __launch_bounds__(512) void ln_kernel(const u16* __restrict__ x, u16* __restrict__ xn, u16* __restrict__ uT,
                                                 int* __restrict__ counters,
    const u16* __restrict__ Are, const u16* __restrict__ Aim,
    const u16* __restrict__ Bre, const u16* __restrict__ Bim, const u16* __restrict__ Cre,
    const u16* __restrict__ Cim, const u16* __restrict__ logdt, u16* __restrict__ KR, float* __restrict__ Ksc,
    const u16* __restrict__ w1, const u16* __restrict__ w2, unsigned* __restrict__ sink) {
  __shared__ __attribute__((aligned(16))) char smem[27648];
  const int tid = threadIdx.x, w = tid >> 6, l = tid & 63;
  if (blockIdx.x >= LN_BLOCKS + D) {
    const int p = blockIdx.x - (LN_BLOCKS + D), xq = (p & 7) >> 1, idx = p >> 3;
    u32x4 v1[6], v2[6];
    const u32x4* w2p = (const u32x4*)(w2 + (size_t)(1152 * xq + 36 * idx) * D);
#pragma unroll
    for (int j = 0; j < 6; ++j) {
      int i = tid + 512 * j;
      if (i >= 18 * 144) i = tid;
      const int r = i / 144, c = i - r * 144;
      v1[j] = *(const u32x4*)(w1 + (size_t)(18 * idx + r) * F + 1152 * xq + c * 8);
      v2[j] = w2p[i];
    }
    u32x4 accv = {0u, 0u, 0u, 0u};
#pragma unroll
    for (int j = 0; j < 6; ++j) accv ^= v1[j] ^ v2[j];
    const unsigned v = accv[0] ^ accv[1] ^ accv[2] ^ accv[3];
    if (v == 0x9e3779b9u) sink[tid] = v;
    return;
  }
  const int bx = blockIdx.x;
  if (bx >= LN_BLOCKS) {
    const int d = bx - LN_BLOCKS;
    float* red = (float*)(smem);
    float* Kd = (float*)(smem + 24576);
    {
      const int idx = l & 15, kk = l >> 4;
      const int nb = d * NS + 16 * w + 4 * kk;
      const u32x2 vAr = *(const u32x2*)(Are + nb), vAi = *(const u32x2*)(Aim + nb), vBr = *(const u32x2*)(Bre + nb),
                  vBi = *(const u32x2*)(Bim + nb), vCr = *(const u32x2*)(Cre + nb), vCi = *(const u32x2*)(Cim + nb);
      const float dt = rbf(__expf(bf2f(logdt[d])));
      const float e0 = (float)(128 * (idx >> 3) + (idx & 7));
      const float e1 = idx < 8 ? (float)(256 + 128 * (idx >> 2) + 2 * (idx & 3)) : (float)(512 + 128 * ((idx >> 1) & 3) + 4 * (idx & 1));
      const float e2 = (float)(1024 + 128 * idx);
      const float eb = (float)(8 * idx);
      float av[3][8], bv[8];
#pragma unroll
      for (int i = 0; i < 4; ++i) {
#define SEL_(v) ((i & 1) ? bfhi(v[i >> 1]) : bflo(v[i >> 1]))
        const float ar = dt * SEL_(vAr), ai = dt * SEL_(vAi), br = dt * SEL_(vBr), bi = dt * SEL_(vBi);
        const float cr = SEL_(vCr), ci = SEL_(vCi);
#undef SEL_
        const cplx cb = {cr * br - ci * bi, cr * bi + ci * br};
        const float arl2 = ar * 1.4426950408889634f;
        const float air = ai * 0.15915494309189535f;
        const cplx A0 = cexp_e(arl2, air, e0), A1 = cexp_e(arl2, air, e1), A2 = cexp_e(arl2, air, e2);
        const cplx B = cmul(cb, cexp_e(arl2, air, eb));
        av[0][2 * i] = A0.re; av[0][2 * i + 1] = A0.im;
        av[1][2 * i] = A1.re; av[1][2 * i + 1] = A1.im;
        av[2][2 * i] = A2.re; av[2][2 * i + 1] = A2.im;
        bv[2 * i] = B.re; bv[2 * i + 1] = -B.im;
      }
      u32x4 ah[3], al[3], bh, bl;
#pragma unroll
      for (int j = 0; j < 4; ++j) {
#pragma unroll
        for (int b = 0; b < 3; ++b) {
          const unsigned h = pack_bf16(av[b][2 * j], av[b][2 * j + 1]);
          ah[b][j] = h;
          al[b][j] = pack_bf16(av[b][2 * j] - bflo(h), av[b][2 * j + 1] - bfhi(h));
        }
        const unsigned h = pack_bf16(bv[2 * j], bv[2 * j + 1]);
        bh[j] = h;
        bl[j] = pack_bf16(bv[2 * j] - bflo(h), bv[2 * j + 1] - bfhi(h));
      }
#pragma unroll
      for (int b = 0; b < 3; ++b) {
        f32x4 ka = {0.f, 0.f, 0.f, 0.f};
        ka = __builtin_amdgcn_mfma_f32_16x16x32_bf16(__builtin_bit_cast(s16x8, al[b]), __builtin_bit_cast(s16x8, bh), ka, 0, 0, 0);
        ka = __builtin_amdgcn_mfma_f32_16x16x32_bf16(__builtin_bit_cast(s16x8, ah[b]), __builtin_bit_cast(s16x8, bl), ka, 0, 0, 0);
        ka = __builtin_amdgcn_mfma_f32_16x16x32_bf16(__builtin_bit_cast(s16x8, ah[b]), __builtin_bit_cast(s16x8, bh), ka, 0, 0, 0);
#pragma unroll
        for (int r = 0; r < 4; ++r) red[(w * 3 + b) * 256 + (4 * kk + r) * 16 + idx] = ka[r];
      }
    }
    __syncthreads();
    for (int e = tid; e < 768; e += 512) {
      float sacc = red[e];
#pragma unroll
      for (int ww = 1; ww < 8; ++ww) sacc += red[ww * 768 + e];
      Kd[e] = sacc;
    }
    __syncthreads();
    {
      float kv[4];
      float mx = 0.f;
#pragma unroll
      for (int h = 0; h < 4; ++h) {
        const int p = 4 * tid + h;
        const int ll = 2047 - p;
        const int lr = (int)rbf((float)ll);
        const int q = (lr >> 3) & 15;
        int row;
        if (lr < 256) row = ((lr >> 7) << 3) + (lr & 7);
        else if (lr < 512) row = 16 + (((lr >> 7) & 1) << 2) + ((lr & 7) >> 1);
        else if (lr < 1024) row = 24 + (((lr >> 7) & 3) << 1) + ((lr & 7) >> 2);
        else row = 32 + ((lr - 1024) >> 7);
        kv[h] = Kd[row * 16 + q];
        mx = fmaxf(mx, fabsf(kv[h]));
      }
#pragma unroll
      for (int o = 1; o < 64; o <<= 1) mx = fmaxf(mx, __shfl_xor(mx, o));
      float* wmx = red;
      if (l == 0) wmx[w] = mx;
      __syncthreads();
      mx = wmx[0];
#pragma unroll
      for (int ww = 1; ww < 8; ++ww) mx = fmaxf(mx, wmx[ww]);
      int ex = (int)((__float_as_uint(mx) >> 23) & 255u);
      int se = 137 - ex;
      se = se > 120 ? 120 : (se < -120 ? -120 : se);
      const float sc = __uint_as_float((unsigned)(127 + se) << 23);
      typedef __attribute__((ext_vector_type(2))) _Float16 h16x2;
      h16x2 p0 = {(_Float16)(kv[0] * sc), (_Float16)(kv[1] * sc)};
      h16x2 p1 = {(_Float16)(kv[2] * sc), (_Float16)(kv[3] * sc)};
      u32x2 o2 = {__builtin_bit_cast(unsigned, p0), __builtin_bit_cast(unsigned, p1)};
      *(u32x2*)(KR + (size_t)d * 2048 + 4 * tid) = o2;
      if (tid == 0) Ksc[d] = __uint_as_float((unsigned)(127 - se) << 23);
    }
    return;
  }
  if (bx < 4) counters[bx * 512 + tid] = 0;
  const int t0 = bx * LN_ROWS;
  const int r = w * 2 + (l >> 5), c = l & 31;
  const bool act_l = c < 24;
  const u32x4* xr = (const u32x4*)(x + (size_t)(t0 + r) * D);
  u32x4 v[3];
#pragma unroll
  for (int i = 0; i < 3; ++i) v[i] = act_l ? xr[c + 24 * i] : (u32x4){0u, 0u, 0u, 0u};
  float s = 0.f;
#pragma unroll
  for (int i = 0; i < 3; ++i)
#pragma unroll
    for (int j = 0; j < 4; ++j) s += bflo(v[i][j]) + bfhi(v[i][j]);
#pragma unroll
  for (int o = 1; o < 32; o <<= 1) s += __shfl_xor(s, o);
  const float mean = s * (1.0f / D);
  float q = 0.f;
#pragma unroll
  for (int i = 0; i < 3; ++i)
#pragma unroll
    for (int j = 0; j < 4; ++j) {
      float a = bflo(v[i][j]) - mean, b = bfhi(v[i][j]) - mean;
      q += a * a + b * b;
    }
  if (!act_l) q = 0.f;
#pragma unroll
  for (int o = 1; o < 32; o <<= 1) q += __shfl_xor(q, o);
  const float var = q * (1.0f / D);
  const float mb = rbf(mean), vb = rbf(var);
  const float den = rbf(sqrtf(rbf(vb + 1e-5f)));
  const float rc = 1.0f / den;
  u32x4* xo = (u32x4*)(xn + (size_t)(t0 + r) * D);
  if (act_l) {
#pragma unroll
    for (int i = 0; i < 3; ++i) {
      u32x4 o;
#pragma unroll
      for (int j = 0; j < 4; ++j) {
        const float da = rbf(bflo(v[i][j]) - mb), db = rbf(bfhi(v[i][j]) - mb);
        float qa = da * rc, qb = db * rc;
        qa = fmaf(fmaf(-qa, den, da), rc, qa);
        qb = fmaf(fmaf(-qb, den, db), rc, qb);
        o[j] = pack_bf16(qa, qb);
      }
      xo[c + 24 * i] = o;
      *(u32x4*)(smem + (r * LN_TS + (c + 24 * i) * 8) * 2) = o;
    }
  }
  __syncthreads();
  const unsigned sbase = (unsigned)(uintptr_t)smem;
  const int g = l >> 4, qq = (l & 15) >> 2, p = l & 3;
  for (int it = w; it < 18; it += 8) {
    const int d0 = it * 32 + (g >> 1) * 16;
    const int rb = (g & 1) * 8;
    s16x4 a = tr_read(sbase + ((rb + qq) * LN_TS + d0 + 4 * p) * 2);
    s16x4 b = tr_read(sbase + ((rb + 4 + qq) * LN_TS + d0 + 4 * p) * 2);
    *(s16x8*)(uT + (size_t)(d0 + (l & 15)) * L + t0 + rb) = cat8(a, b);
  }
}

constexpr int G1_A_BYTES = 128 * 32 * 2;
constexpr int G1_B_BYTES = 32 * 288 * 2;
constexpr int G1_STAGE = G1_A_BYTES + G1_B_BYTES;
constexpr int CV2_UC = 4160;
constexpr int CV2_K = 2 * CV2_UC;
constexpr int CV2_BYTES = CV2_K + 4672 + 32;
constexpr int G1_LDS = 4 * G1_STAGE + 3 * CV2_BYTES;

constexpr int CV_UC_STRIDE = 4160;
constexpr int CV_K_OFF = 4 * CV_UC_STRIDE;
constexpr int CV_K_BYTES = 2336 * 2;
constexpr int CV_KD_OFF = CV_K_OFF + 2 * CV_K_BYTES;
constexpr int CV_CH_STRIDE = 32768;
constexpr int CV_BLOCKS = D / 3;

__device__ __forceinline__ float mish_f(float h) {
  float t = fminf(h * 1.4426950408889634f, 43.f);
  float e = __builtin_amdgcn_exp2f(t);
  float a = e + 2.f;
  float n = e * a;
  float n2 = fmaf(e, a, 2.f);
  return h * (n * __builtin_amdgcn_rcpf(n2));
}

__device__ __forceinline__ unsigned mish2(float h0, float h1) {
  const f32x2 h = {h0, h1};
  f32x2 t = h * 1.4426950408889634f;
  f32x2 e = {__builtin_amdgcn_exp2f(fminf(t[0], 43.f)), __builtin_amdgcn_exp2f(fminf(t[1], 43.f))};
  f32x2 a = e + 2.f;
  f32x2 n = e * a;
  f32x2 n2 = n + 2.f;
  f32x2 r = {__builtin_amdgcn_rcpf(n2[0]), __builtin_amdgcn_rcpf(n2[1])};
  f32x2 o = h * (n * r);
  return pack_bf16(o[0], o[1]);
}

__global__ __launch_bounds__(768, 3) void gemm1_conv_kernel(
    const u16* __restrict__ xn, const u16* __restrict__ w1, const u16* __restrict__ b1, u16* __restrict__ act,
    const u16* __restrict__ uT, const u16* __restrict__ KR, const float* __restrict__ Ksc, u16* __restrict__ yT) {
  extern __shared__ __attribute__((aligned(16))) char smem[];
  const unsigned sbase = (unsigned)(uintptr_t)smem;
  const int tid = threadIdx.x, l = tid & 63;
  const int w = __builtin_amdgcn_readfirstlane(tid >> 6);
  const int g = l >> 4, li = l & 15;
  if (blockIdx.x < 256) {
    const int b = blockIdx.x, xcd = b & 7, idx = b >> 3;
    const int mi = 8 * (xcd & 1) + (idx & 7), ni = 4 * (xcd >> 1) + (idx >> 3);
    const int m0 = mi * 128, n0 = ni * 288;
    if (w >= 8) {
      const int lw = w - 8;
      int aoff[2], boff[5];
#pragma unroll
      for (int j = 0; j < 2; ++j) {
        const int p = (lw + 4 * j) * 64 + l;
        const int row = p >> 2, chp = p & 3;
        const int f = (4 - ((row >> 2) & 3)) & 3;
        aoff[j] = (m0 + row) * D + ((chp ^ f) * 8);
      }
#pragma unroll
      for (int j = 0; j < 5; ++j) {
        int ins = lw + 4 * j;
        if (ins >= 18) ins = 0;
        const int p = ins * 64 + l;
        const int row = p / 36, chp = p - row * 36;
        boff[j] = row * F + n0 + ((chp ^ (((row >> 3) & 1) << 1)) * 8);
      }
      auto stage = [&](int t, int buf) {
        const unsigned sb = sbase + buf * G1_STAGE;
#pragma unroll
        for (int j = 0; j < 2; ++j) glds16(xn + aoff[j] + t * 32, sb + (lw + 4 * j) * 1024);
#pragma unroll
        for (int j = 0; j < 4; ++j) glds16(w1 + boff[j] + (size_t)t * 32 * F, sb + G1_A_BYTES + (lw + 4 * j) * 1024);
        if (lw < 2) glds16(w1 + boff[4] + (size_t)t * 32 * F, sb + G1_A_BYTES + (lw + 16) * 1024);
      };
#define LVMWAIT(big, small) do { if (lw < 2) asm volatile("s_waitcnt vmcnt(" #big ")" ::: "memory"); else asm volatile("s_waitcnt vmcnt(" #small ")" ::: "memory"); } while (0)
      typedef __attribute__((ext_vector_type(8))) _Float16 h16x8;
      typedef __attribute__((ext_vector_type(2))) _Float16 h16x2;
      __builtin_amdgcn_s_setprio(1);
      const int d = b + 256 * lw;
      const bool hasc = d < D;
      const int coff = 4 * G1_STAGE + lw * CV2_BYTES;
      char* cs = smem + coff;
      const unsigned cbase = sbase + coff;
      float ysc = 0.f;
      {
        u32x4 cu[4], ck[4];
        unsigned cun[4];
        if (hasc) {
#pragma unroll
          for (int i = 0; i < 4; ++i) {
            const int e = 8 * (l + 64 * i);
            cu[i] = *(const u32x4*)(uT + (size_t)d * L + e);
            cun[i] = (e + 8 < L) ? *(const unsigned*)(uT + (size_t)d * L + e + 8) : 0u;
            ck[i] = *(const u32x4*)(KR + (size_t)d * 2048 + e);
          }
          ysc = Ksc[d];
        }
        stage(0, 0);
        stage(1, 1);
        stage(2, 2);
        if (hasc) {
          LVMWAIT(21, 18);
          asm volatile("" : "+v"(cu[0]), "+v"(cu[1]), "+v"(cu[2]), "+v"(cu[3]), "+v"(ck[0]), "+v"(ck[1]), "+v"(ck[2]), "+v"(ck[3]),
                       "+v"(cun[0]), "+v"(cun[1]), "+v"(cun[2]), "+v"(cun[3]), "+v"(ysc));
#pragma unroll
          for (int i = 0; i < 4; ++i) {
            const int tc = l + 64 * i;
            *(u32x4*)(cs + CV2_K + tc * 16) = ck[i];
            unsigned wv[5];
#pragma unroll
            for (int j = 0; j < 5; ++j) {
              const unsigned src = j < 4 ? cu[i][j] : cun[i];
              h16x2 hv = {(_Float16)bflo(src), (_Float16)bfhi(src)};
              wv[j] = __builtin_bit_cast(unsigned, hv);
            }
            u32x4 c0 = {wv[0], wv[1], wv[2], wv[3]}, c1;
#pragma unroll
            for (int j = 0; j < 4; ++j) c1[j] = __builtin_amdgcn_alignbyte(wv[j + 1], wv[j], 2);
            char* dst = cs + (8 * tc + 16) * 2;
            *(u32x4*)(dst) = c0;
            *(u32x4*)(dst + CV2_UC) = c1;
            if (i == 0) {
              const unsigned h0 = __shfl(wv[0], 0);
              if (l < 32) {
                const int c = l >> 4, ii = l & 15;
                *(u16*)(cs + c * CV2_UC + ii * 2) = (u16)((c == 1 && ii == 15) ? (h0 & 0xffffu) : 0u);
              }
            }
          }
          if (l < 36) *(u32x4*)(cs + CV2_K + 4096 + l * 16) = (u32x4){0u, 0u, 0u, 0u};
        }
      }
      f32x4 cacc[8];
#pragma unroll
      for (int j = 0; j < 8; ++j) cacc[j] = (f32x4){0.f, 0.f, 0.f, 0.f};
      const int cc = (li + 1) & 1;
      const unsigned ua = cbase + cc * CV2_UC + (li + 1 - cc + 8 * g) * 2;
      const unsigned kb = cbase + CV2_K + (2032 - 16 * li + 8 * g) * 2;
      LVMWAIT(14, 12);
      __builtin_amdgcn_s_barrier();
      for (int rho = 0; rho < 8; ++rho) {
        const int t = 2 * rho;
        u32x4 A[8];
        s16x8 Bh[8];
        LVMWAIT(7, 6);
        __builtin_amdgcn_s_barrier();
        stage(t + 3, (t + 3) & 3);
        if (hasc) {
#pragma unroll
          for (int s = 0; s < 8; ++s) {
            const unsigned ad = ua + (32 * (8 * s + rho)) * 2;
#pragma unroll
            for (int j = 0; j < 4; ++j) A[s][j] = *(LDS_AS unsigned*)(uintptr_t)(ad + 4 * j);
          }
#pragma unroll
          for (int dl = 0; dl < 8; ++dl) Bh[dl] = lds_read128(kb + (32 * rho - 256 * dl) * 2);
#pragma unroll
          for (int j = 0; j < 6; ++j)
#pragma unroll
            for (int s = 0; s <= j; ++s)
              cacc[j] = __builtin_amdgcn_mfma_f32_16x16x32_f16(__builtin_bit_cast(h16x8, A[s]), __builtin_bit_cast(h16x8, Bh[j - s]), cacc[j], 0, 0, 0);
        }
        LVMWAIT(7, 6);
        __builtin_amdgcn_s_barrier();
        if (t + 4 < 18) stage(t + 4, (t + 4) & 3);
        if (hasc) {
#pragma unroll
          for (int j = 6; j < 8; ++j)
#pragma unroll
            for (int s = 0; s <= j; ++s)
              cacc[j] = __builtin_amdgcn_mfma_f32_16x16x32_f16(__builtin_bit_cast(h16x8, A[s]), __builtin_bit_cast(h16x8, Bh[j - s]), cacc[j], 0, 0, 0);
        }
      }
      asm volatile("s_waitcnt vmcnt(0)" ::: "memory");
      __builtin_amdgcn_s_barrier();
      if (hasc) {
#pragma unroll
        for (int j = 0; j < 8; ++j) {
          const f32x4 sv = cacc[j] * ysc;
          u32x2 o = {pack_bf16(sv[0], sv[1]), pack_bf16(sv[2], sv[3])};
          *(u32x2*)(yT + (size_t)d * L + 256 * j + 16 * li + 4 * g) = o;
        }
      }
    } else {
      const int wm = w & 3, wn = w >> 2;
      const int fa = (4 - ((li >> 2) & 3)) & 3;
      const unsigned a_rd = (32 * wm + li) * 64 + ((g ^ fa) * 16);
      const int q = li >> 2, p4 = li & 3;
      const unsigned b_rd = G1_A_BYTES + (8 * g + q) * 576 + (p4 & 1) * 8;
      const int bsw = (g & 1) << 1;
      f32x4 acc[9][2];
#pragma unroll
      for (int nt = 0; nt < 9; ++nt) {
        const u32x2 bb = *(const u32x2*)(b1 + n0 + wn * 144 + nt * 16 + 4 * g);
        const f32x4 bv = {bflo(bb[0]), bfhi(bb[0]), bflo(bb[1]), bfhi(bb[1])};
#pragma unroll
        for (int mt = 0; mt < 2; ++mt) acc[nt][mt] = bv;
      }
      const unsigned bE0 = b_rd + (p4 >> 1) * 16 + wn * 288 + (wn ? -bsw * 16 : bsw * 16);
      const unsigned bO0 = b_rd + (p4 >> 1) * 16 + wn * 288 + (wn ? bsw * 16 : -bsw * 16);
      s16x8 xa0[2], xa1[2];
      s16x4 wl0[9], wh0[9], wl1[9], wh1[9];
      __builtin_amdgcn_s_barrier();
      {
        const unsigned pA = sbase + a_rd, pE = sbase + bE0, pO = sbase + bO0;
        RDSET(xa0, wl0, wh0, pA, 1024, pE, pO);
      }
      for (int t = 0; t < 16; t += 2) {
        {
          __builtin_amdgcn_s_barrier();
          const unsigned sbn = sbase + ((t + 1) & 3) * G1_STAGE;
          const unsigned pA = sbn + a_rd, pE = sbn + bE0, pO = sbn + bO0;
          WAITSET(0, xa0, wl0, wh0);
          RDSET(xa1, wl1, wh1, pA, 1024, pE, pO);
          MMASET(xa0, wl0, wh0);
        }
        {
          __builtin_amdgcn_s_barrier();
          const unsigned sbn = sbase + ((t + 2) & 3) * G1_STAGE;
          const unsigned pA = sbn + a_rd, pE = sbn + bE0, pO = sbn + bO0;
          WAITSET(0, xa1, wl1, wh1);
          RDSET(xa0, wl0, wh0, pA, 1024, pE, pO);
          MMASET(xa1, wl1, wh1);
        }
      }
      {
        __builtin_amdgcn_s_barrier();
        const unsigned sbn = sbase + (17 & 3) * G1_STAGE;
        const unsigned pA = sbn + a_rd, pE = sbn + bE0, pO = sbn + bO0;
        WAITSET(0, xa0, wl0, wh0);
        RDSET(xa1, wl1, wh1, pA, 1024, pE, pO);
        MMASET(xa0, wl0, wh0);
        WAITSET(0, xa1, wl1, wh1);
        MMASET(xa1, wl1, wh1);
      }
      int tz = threadIdx.x;
      asm volatile("" : "+v"(tz));
      const int li2 = tz & 15, g2 = (tz >> 4) & 3;
#pragma unroll
      for (int mt = 0; mt < 2; ++mt) {
        const int row = 32 * wm + 16 * mt + li2;
        const int sw = (row >> 1) & 7;
#pragma unroll
        for (int nt = 0; nt < 9; ++nt) {
          const int n = n0 + wn * 144 + nt * 16 + 4 * g2;
          const int kt = n >> 6, ch = (n >> 3) & 7;
          u16* dst = act + ((size_t)(mi * 72 + kt) * 128 + row) * 64 + ((ch ^ sw) * 8) + (n & 7);
          u32x2 o;
          o[0] = mish2(acc[nt][mt][0], acc[nt][mt][1]);
          o[1] = mish2(acc[nt][mt][2], acc[nt][mt][3]);
          *(u32x2*)(dst) = o;
          if (nt % 3 == 2) __builtin_amdgcn_sched_barrier(0);
        }
      }
    }
  }
}

constexpr int G2_A_BYTES = 128 * 64 * 2;
constexpr int G2_B_BYTES = 32 * 576;
constexpr int G2_STAGE = G2_A_BYTES + G2_B_BYTES;
constexpr int G2_NST = 4;
constexpr int G2_LDS = G2_NST * G2_STAGE;

__global__ __launch_bounds__(768, 3) void gemm2_kernel(
    const u16* __restrict__ act, const u16* __restrict__ w2, float* __restrict__ slabs, int* __restrict__ counters,
    const u16* __restrict__ x, const u16* __restrict__ xn, const u16* __restrict__ yT, const u16* __restrict__ Dskip,
    u16* __restrict__ out) {
  extern __shared__ __attribute__((aligned(16))) char smem[];
  STAMP(0);
  const unsigned sbase = (unsigned)(uintptr_t)smem;
  const int tid = threadIdx.x, l = tid & 63;
  const int w = __builtin_amdgcn_readfirstlane(tid >> 6);
  const int g = l >> 4, li = l & 15;
  const int b = blockIdx.x, xcd = b & 7, idx = b >> 3;
  const int mi = 8 * (xcd & 1) + (idx & 7), ks = xcd >> 1, ni = idx >> 3;
  const int m0 = mi * 128, n0 = ni * 144, k0 = ks * 1152;
  const int wm = w & 3, wk = (w >> 2) & 1;
  f32x4 acc[9][2];
#pragma unroll
  for (int nt = 0; nt < 9; ++nt)
#pragma unroll
    for (int mt = 0; mt < 2; ++mt) acc[nt][mt] = (f32x4){0.f, 0.f, 0.f, 0.f};
  const int R0 = m0 + 32 * ks;
  const int uh = w >= 5 ? 1 : 0, uj = w - 5 * uh;
  const int ntile = (w >= 10) ? 0 : (uj < 4 ? 2 : 1);
  u32x2 xv[2], nv[2], dv[2];
  unsigned yv[2][4];
  auto load_ops = [&]() {
#pragma unroll
    for (int i = 0; i < 2; ++i) {
      int nt = 2 * uj + i;
      nt = nt > 8 ? 8 : nt;
      const int row = R0 + 16 * uh + li, col = n0 + nt * 16 + 4 * g;
      const size_t go = (size_t)row * D + col;
      xv[i] = *(const u32x2*)(x + go);
      nv[i] = *(const u32x2*)(xn + go);
      dv[i] = *(const u32x2*)(Dskip + col);
#pragma unroll
      for (int e = 0; e < 4; ++e) yv[i][e] = yT[(size_t)(col + e) * L + row];
    }
  };
  if (w >= 8) {
    const int lw = w - 8;
    const u16* aptr = act + (size_t)(mi * 72 + ks * 18) * 8192 + (lw * 64 + l) * 8;
    int boff[5];
#pragma unroll
    for (int j = 0; j < 5; ++j) {
      int ins = lw + 4 * j;
      if (ins >= 18) ins = 0;
      const int p = ins * 64 + l;
      const int row = p / 36, chp = p - row * 36;
      const int ch = chp ^ (((row >> 3) & 1) << 1);
      const int half = ch >= 18 ? 1 : 0;
      boff[j] = (k0 + row + 32 * half) * D + n0 + (ch - 18 * half) * 8;
    }
    auto stage = [&](int t, int buf) {
      const unsigned sb = sbase + buf * G2_STAGE;
#pragma unroll
      for (int j = 0; j < 4; ++j) glds16(aptr + t * 8192 + j * 2048, sb + (lw + 4 * j) * 1024);
#pragma unroll
      for (int j = 0; j < 4; ++j) glds16(w2 + boff[j] + t * 64 * D, sb + G2_A_BYTES + (lw + 4 * j) * 1024);
      if (lw < 2) glds16(w2 + boff[4] + t * 64 * D, sb + G2_A_BYTES + (lw + 16) * 1024);
    };
    stage(0, 0);
    stage(1, 1);
    stage(2, 2);
    LVMWAIT(18, 16);
    __builtin_amdgcn_s_barrier();
    for (int t = 0; t < 15; ++t) {
      LVMWAIT(9, 8);
      __builtin_amdgcn_s_barrier();
      stage(t + 3, (t + 3) & 3);
    }
    asm volatile("" ::: "memory");
    load_ops();
    asm volatile("" ::: "memory");
    LVMWAIT(23, 22);
    __builtin_amdgcn_s_barrier();
    asm volatile("s_waitcnt vmcnt(14)" ::: "memory");
    __builtin_amdgcn_s_barrier();
  } else {
    const unsigned a_rd = (32 * wm + li) * 128 + (((4 * wk + g) ^ ((li >> 1) & 7)) * 16);
    const int q = li >> 2, p4 = li & 3;
    const unsigned b_rd = G2_A_BYTES + (8 * g + q) * 576 + (p4 & 1) * 8;
    const int bsw = (g & 1) << 1;
    const unsigned bE0 = b_rd + (p4 >> 1) * 16 + wk * 288 + (wk ? -bsw * 16 : bsw * 16);
    const unsigned bO0 = b_rd + (p4 >> 1) * 16 + wk * 288 + (wk ? bsw * 16 : -bsw * 16);
    s16x8 xa0[2], xa1[2];
    s16x4 wl0[9], wh0[9], wl1[9], wh1[9];
    __builtin_amdgcn_s_barrier();
    STAMP(1);
    {
      const unsigned pA = sbase + a_rd, pE = sbase + bE0, pO = sbase + bO0;
      RDSET(xa0, wl0, wh0, pA, 2048, pE, pO);
    }
    asm volatile("" ::: "memory");
    load_ops();
    asm volatile("" ::: "memory");
    for (int t = 0; t < 16; t += 2) {
      {
        __builtin_amdgcn_s_barrier();
        const unsigned sbn = sbase + ((t + 1) & 3) * G2_STAGE;
        const unsigned pA = sbn + a_rd, pE = sbn + bE0, pO = sbn + bO0;
        WAITSET(0, xa0, wl0, wh0);
        RDSET(xa1, wl1, wh1, pA, 2048, pE, pO);
        MMASET(xa0, wl0, wh0);
      }
      {
        __builtin_amdgcn_s_barrier();
        const unsigned sbn = sbase + ((t + 2) & 3) * G2_STAGE;
        const unsigned pA = sbn + a_rd, pE = sbn + bE0, pO = sbn + bO0;
        WAITSET(0, xa1, wl1, wh1);
        RDSET(xa0, wl0, wh0, pA, 2048, pE, pO);
        MMASET(xa1, wl1, wh1);
      }
    }
    {
      __builtin_amdgcn_s_barrier();
      const unsigned sbn = sbase + (17 & 3) * G2_STAGE;
      const unsigned pA = sbn + a_rd, pE = sbn + bE0, pO = sbn + bO0;
      WAITSET(0, xa0, wl0, wh0);
      RDSET(xa1, wl1, wh1, pA, 2048, pE, pO);
      MMASET(xa0, wl0, wh0);
      WAITSET(0, xa1, wl1, wh1);
      MMASET(xa1, wl1, wh1);
    }
  }
  __syncthreads();
  STAMP(2);
  f32x4 res[9];
  {
    f32x4* ex = (f32x4*)smem;
    if (w < 4) {
#pragma unroll
      for (int nt = 0; nt < 9; ++nt) ex[(w * 9 + nt) * 64 + l] = acc[nt][1];
    } else if (w < 8) {
#pragma unroll
      for (int nt = 0; nt < 9; ++nt) ex[(w * 9 + nt) * 64 + l] = acc[nt][0];
    }
    __syncthreads();
    const int pw = (w ^ 4) & 7;
    if (w < 4) {
#pragma unroll
      for (int nt = 0; nt < 9; ++nt) res[nt] = acc[nt][0] + ex[(pw * 9 + nt) * 64 + l];
    } else {
#pragma unroll
      for (int nt = 0; nt < 9; ++nt) res[nt] = acc[nt][1] + ex[(pw * 9 + nt) * 64 + l];
    }
  }
  const int grp = mi * 4 + ni;
  {
    float* gslab = slabs + (size_t)grp * (147456 / 4);
    auto rs = __builtin_amdgcn_make_buffer_rsrc((void*)gslab, 0, 147456, 0x00020000);
    __syncthreads();
    f32x4* own = (f32x4*)smem;
    int* flags = counters + grp * 32;
    if (w >= 8) {
    } else if (wm == ks) {
#pragma unroll
      for (int nt = 0; nt < 9; ++nt) own[(wk * 9 + nt) * 64 + l] = res[nt];
    } else {
      const int base = ((wm * 4 + ks) * 2 + wk) * 4608;
#pragma unroll
      for (int j = 0; j < 4; ++j) {
        const u32x4 pk = {pack_bf16(res[2 * j][0], res[2 * j][1]), pack_bf16(res[2 * j][2], res[2 * j][3]),
                          pack_bf16(res[2 * j + 1][0], res[2 * j + 1][1]), pack_bf16(res[2 * j + 1][2], res[2 * j + 1][3])};
        __builtin_amdgcn_raw_buffer_store_b128(pk, rs, base + j * 1024 + l * 16, 0, 16);
      }
      const u32x2 pk8 = {pack_bf16(res[8][0], res[8][1]), pack_bf16(res[8][2], res[8][3])};
      __builtin_amdgcn_raw_buffer_store_b64(pk8, rs, base + 4096 + l * 8, 0, 16);
      asm volatile("s_waitcnt vmcnt(0)" ::: "memory");
      if (l == 0) __hip_atomic_store(&flags[(wm * 4 + ks) * 2 + wk], 1, __ATOMIC_RELAXED, __HIP_MEMORY_SCOPE_AGENT);
    }
    STAMP(3);
    asm volatile("s_waitcnt lgkmcnt(0)" ::: "memory");
    __builtin_amdgcn_s_barrier();
    if (ntile > 0) {
      {
        const int src = l & 3;
        const bool need = (l < 4) && (src != ks);
        for (int spin = 0; spin < (1 << 20); ++spin) {
          int v = 1;
          if (need) v = __hip_atomic_load(&flags[(ks * 4 + src) * 2 + uh], __ATOMIC_RELAXED, __HIP_MEMORY_SCOPE_AGENT);
          if (__all(v != 0)) break;
          __builtin_amdgcn_s_sleep(1);
        }
        asm volatile("" ::: "memory");
      }
      STAMP(4);
      u32x4 pv[3];
#pragma unroll
      for (int k = 0; k < 3; ++k) {
        const int src = k + (k >= ks ? 1 : 0);
        const int base = ((ks * 4 + src) * 2 + uh) * 4608;
        if (uj < 4) pv[k] = __builtin_amdgcn_raw_buffer_load_b128(rs, base + uj * 1024 + l * 16, 0, 16);
        else { const u32x2 t2 = __builtin_amdgcn_raw_buffer_load_b64(rs, base + 4096 + l * 8, 0, 16); pv[k] = (u32x4){t2[0], t2[1], 0u, 0u}; }
      }
#pragma unroll
      for (int i = 0; i < 2; ++i) {
        if (i < ntile) {
          const int nt = 2 * uj + i;
          const int row = R0 + 16 * uh + li, col = n0 + nt * 16 + 4 * g;
          f32x4 parts[4];
          const f32x4 mine = own[(uh * 9 + nt) * 64 + l];
#pragma unroll
          for (int k = 0; k < 4; ++k) {
            const int kk = k - (k > ks ? 1 : 0);
            const u32x4 pq = pv[kk < 3 ? kk : 2];
            const f32x4 rem = {bflo(pq[2 * i]), bfhi(pq[2 * i]), bflo(pq[2 * i + 1]), bfhi(pq[2 * i + 1])};
            parts[k] = (k == ks) ? mine : rem;
          }
          const f32x4 sum = (parts[0] + parts[1]) + (parts[2] + parts[3]);
          float o[4];
#pragma unroll
          for (int e = 0; e < 4; ++e) {
            const float xf = (e & 1) ? bfhi(xv[i][e >> 1]) : bflo(xv[i][e >> 1]);
            const float nf = (e & 1) ? bfhi(nv[i][e >> 1]) : bflo(nv[i][e >> 1]);
            const float df = (e & 1) ? bfhi(dv[i][e >> 1]) : bflo(dv[i][e >> 1]);
            const float ya = rbf(bflo(yv[i][e]) + rbf(nf * df));
            o[e] = rbf(xf + ya) + rbf(sum[e]);
          }
          u32x2 ov = {pack_bf16(o[0], o[1]), pack_bf16(o[2], o[3])};
          *(u32x2*)(out + (size_t)row * D + col) = ov;
        }
      }
    }
  }
}

extern "C" void kernel_launch(void* const* d_in, const int* in_sizes, int n_in,
                              void* d_out, int out_size, void* d_ws, size_t ws_size,
                              hipStream_t stream) {
  const u16* x = (const u16*)d_in[0];
  const u16* Are = (const u16*)d_in[1];
  const u16* Aim = (const u16*)d_in[2];
  const u16* Bre = (const u16*)d_in[3];
  const u16* Bim = (const u16*)d_in[4];
  const u16* Cre = (const u16*)d_in[5];
  const u16* Cim = (const u16*)d_in[6];
  const u16* logdt = (const u16*)d_in[7];
  const u16* Dskip = (const u16*)d_in[8];
  const u16* w1 = (const u16*)d_in[10];
  const u16* b1 = (const u16*)d_in[11];
  const u16* w2 = (const u16*)d_in[12];
  u16* out = (u16*)d_out;
  char* ws = (char*)d_ws;
  constexpr size_t SZ = (size_t)L * D * 2;
  int* counters = (int*)(ws);
  float* Ksc = (float*)(ws + 16384);
  unsigned* sinkp = (unsigned*)(ws + 32768);
  u16* xn = (u16*)(ws + 65536);
  u16* uT = (u16*)(ws + 65536 + SZ);
  u16* yT = (u16*)(ws + 65536 + 2 * SZ);
  u16* KR = (u16*)(ws + 65536 + 3 * SZ);
  float* slabs = (float*)(ws + 65536 + 4 * SZ);
  u16* act = (u16*)(ws + 65536 + 4 * SZ + 64 * 147456);
  hipFuncSetAttribute((const void*)gemm1_conv_kernel, hipFuncAttributeMaxDynamicSharedMemorySize, G1_LDS);
  hipFuncSetAttribute((const void*)gemm2_kernel, hipFuncAttributeMaxDynamicSharedMemorySize, G2_LDS);
  ln_kernel<<<LN_BLOCKS + D + 256, 512, 0, stream>>>(x, xn, uT, counters, Are, Aim, Bre, Bim, Cre, Cim, logdt, KR, Ksc, w1, w2, sinkp);
  gemm1_conv_kernel<<<256, 768, G1_LDS, stream>>>(xn, w1, b1, act, uT, KR, Ksc, yT);
  gemm2_kernel<<<256, 768, G2_LDS, stream>>>(act, w2, slabs, counters, x, xn, yT, Dskip, out);
}
```

```cpp
#include <hip/hip_runtime.h>
#include <hip/hip_bf16.h>
#include <stdint.h>

typedef unsigned short u16;
typedef __attribute__((ext_vector_type(4))) short s16x4;
typedef __attribute__((ext_vector_type(8))) short s16x8;
typedef __attribute__((ext_vector_type(4))) float f32x4;
typedef __attribute__((ext_vector_type(2))) float f32x2;
typedef __attribute__((ext_vector_type(4))) unsigned u32x4;
typedef __attribute__((ext_vector_type(2))) unsigned u32x2;
typedef __attribute__((ext_vector_type(2))) __bf16 bf16x2_t;
#define LDS_AS __attribute__((address_space(3)))

constexpr int L = 2048, D = 576, NS = 128, F = 4608;


__device__ __forceinline__ float bf2f(u16 v) { return __uint_as_float(((unsigned)v) << 16); }
__device__ __forceinline__ float bflo(unsigned v) { return __uint_as_float(v << 16); }
__device__ __forceinline__ float bfhi(unsigned v) { return __uint_as_float(v & 0xffff0000u); }
__device__ __forceinline__ unsigned pack_bf16(float a, float b) {
  f32x2 v = {a, b};
  bf16x2_t r = __builtin_convertvector(v, bf16x2_t);
  return __builtin_bit_cast(unsigned, r);
}
__device__ __forceinline__ float rbf(float f) { return __uint_as_float(pack_bf16(f, 0.f) << 16); }

__device__ __forceinline__ void glds16(const void* g, unsigned lds_byte_addr_uniform) {
  __builtin_amdgcn_global_load_lds((const unsigned*)g, (LDS_AS unsigned*)(uintptr_t)lds_byte_addr_uniform, 16, 0, 0);
}
__device__ __forceinline__ s16x4 tr_read(unsigned lds_addr) {
  return __builtin_amdgcn_ds_read_tr16_b64_v4i16((LDS_AS s16x4*)(uintptr_t)lds_addr);
}
__device__ __forceinline__ s16x8 lds_read128(unsigned lds_addr) {
  return *(LDS_AS s16x8*)(uintptr_t)lds_addr;
}
__device__ __forceinline__ s16x4 lds_read64(unsigned lds_addr) {
  return *(LDS_AS s16x4*)(uintptr_t)lds_addr;
}
__device__ __forceinline__ s16x8 cat8(s16x4 a, s16x4 b) { return __builtin_shufflevector(a, b, 0, 1, 2, 3, 4, 5, 6, 7); }

#define RD_TR(dst, addr, off) asm volatile("ds_read_b64_tr_b16 %0, %1 offset:%2" : "=v"(dst) : "v"(addr), "i"(off))
#define RD128(dst, addr, off) asm volatile("ds_read_b128 %0, %1 offset:%2" : "=v"(dst) : "v"(addr), "i"(off))

#define RDSET(XS, WL, WH, pA, AMT, pE, pO) do { \
    RD128(XS[0], pA, 0); RD128(XS[1], pA, AMT); \
    _Pragma("unroll") for (int nt_ = 0; nt_ < 9; nt_ += 1) { \
      if (nt_ & 1) { RD_TRX(WL[nt_], pO, nt_ * 32); RD_TRX(WH[nt_], pO, nt_ * 32 + 2304); } \
      else { RD_TRX(WL[nt_], pE, nt_ * 32); RD_TRX(WH[nt_], pE, nt_ * 32 + 2304); } } } while (0)
#define WAITSET(n, XS, WL, WH) asm volatile("s_waitcnt lgkmcnt(" #n ")" : "+v"(XS[0]), "+v"(XS[1]), \
    "+v"(WL[0]), "+v"(WL[1]), "+v"(WL[2]), "+v"(WL[3]), "+v"(WL[4]), "+v"(WL[5]), "+v"(WL[6]), "+v"(WL[7]), "+v"(WL[8]), \
    "+v"(WH[0]), "+v"(WH[1]), "+v"(WH[2]), "+v"(WH[3]), "+v"(WH[4]), "+v"(WH[5]), "+v"(WH[6]), "+v"(WH[7]), "+v"(WH[8]))
#define MMASET(XS, WL, WH) do { _Pragma("unroll") for (int nt_ = 0; nt_ < 9; ++nt_) { s16x8 wb_ = cat8(WL[nt_], WH[nt_]); \
    acc[nt_][0] = __builtin_amdgcn_mfma_f32_16x16x32_bf16(wb_, XS[0], acc[nt_][0], 0, 0, 0); \
    acc[nt_][1] = __builtin_amdgcn_mfma_f32_16x16x32_bf16(wb_, XS[1], acc[nt_][1], 0, 0, 0); } } while (0)
template <int OFF> __device__ __forceinline__ s16x4 rd_tr_t(unsigned addr) { s16x4 r; asm volatile("ds_read_b64_tr_b16 %0, %1 offset:%2" : "=v"(r) : "v"(addr), "i"(OFF)); return r; }
#define RD_TRX(dst, addr, off) dst = rd_tr_sel(addr, off)
__device__ __forceinline__ s16x4 rd_tr_sel(unsigned addr, int off) {
  switch (off) {
#define C_(o) case o: return rd_tr_t<o>(addr);
    C_(0) C_(32) C_(64) C_(96) C_(128) C_(160) C_(192) C_(224) C_(256)
    C_(2304) C_(2336) C_(2368) C_(2400) C_(2432) C_(2464) C_(2496) C_(2528) C_(2560)
#undef C_
  }
  return rd_tr_t<0>(addr);
}

constexpr int LN_ROWS = 16;
constexpr int LN_TS = 592;

struct cplx { float re, im; };
__device__ __forceinline__ cplx cmul(cplx a, cplx b) { return {a.re * b.re - a.im * b.im, a.re * b.im + a.im * b.re}; }
__device__ __forceinline__ cplx csq(cplx a) { return {a.re * a.re - a.im * a.im, 2.f * a.re * a.im}; }
__device__ __forceinline__ cplx cexp_e(float arl2, float air, float e) {
  float m = __builtin_amdgcn_exp2f(arl2 * e);
  float th = air * e;
  th = __builtin_amdgcn_fractf(th);
  float s = __builtin_amdgcn_sinf(th);
  float c = __builtin_amdgcn_cosf(th);
  return {m * c, m * s};
}

constexpr int LN_BLOCKS = L / LN_ROWS;
__global__ __launch_bounds__(512) void ln_kernel(const u16* __restrict__ x, u16* __restrict__ xn, u16* __restrict__ uT,
                                                 int* __restrict__ counters,
    const u16* __restrict__ Are, const u16* __restrict__ Aim,
    const u16* __restrict__ Bre, const u16* __restrict__ Bim, const u16* __restrict__ Cre,
    const u16* __restrict__ Cim, const u16* __restrict__ logdt, u16* __restrict__ KR, float* __restrict__ Ksc,
    const u16* __restrict__ w1, const u16* __restrict__ w2, unsigned* __restrict__ sink) {
  __shared__ __attribute__((aligned(16))) char smem[27648];
  const int tid = threadIdx.x, w = tid >> 6, l = tid & 63;
  if (blockIdx.x >= LN_BLOCKS + D) {
    const int p = blockIdx.x - (LN_BLOCKS + D), xq = (p & 7) >> 1, idx = p >> 3;
    u32x4 v1[6], v2[6];
    const u32x4* w2p = (const u32x4*)(w2 + (size_t)(1152 * xq + 36 * idx) * D);
#pragma unroll
    for (int j = 0; j < 6; ++j) {
      int i = tid + 512 * j;
      if (i >= 18 * 144) i = tid;
      const int r = i / 144, c = i - r * 144;
      v1[j] = *(const u32x4*)(w1 + (size_t)(18 * idx + r) * F + 1152 * xq + c * 8);
      v2[j] = w2p[i];
    }
    u32x4 accv = {0u, 0u, 0u, 0u};
#pragma unroll
    for (int j = 0; j < 6; ++j) accv ^= v1[j] ^ v2[j];
    const unsigned v = accv[0] ^ accv[1] ^ accv[2] ^ accv[3];
    if (v == 0x9e3779b9u) sink[tid] = v;
    return;
  }
  const int bx = blockIdx.x;
  if (bx >= LN_BLOCKS) {
    const int d = bx - LN_BLOCKS;
    float* red = (float*)(smem);
    float* Kd = (float*)(smem + 24576);
    {
      const int idx = l & 15, kk = l >> 4;
      const int nb = d * NS + 16 * w + 4 * kk;
      const u32x2 vAr = *(const u32x2*)(Are + nb), vAi = *(const u32x2*)(Aim + nb), vBr = *(const u32x2*)(Bre + nb),
                  vBi = *(const u32x2*)(Bim + nb), vCr = *(const u32x2*)(Cre + nb), vCi = *(const u32x2*)(Cim + nb);
      const float dt = rbf(expf(bf2f(logdt[d])));
      const float e0 = (float)(128 * (idx >> 3) + (idx & 7));
      const float e1 = idx < 8 ? (float)(256 + 128 * (idx >> 2) + 2 * (idx & 3)) : (float)(512 + 128 * ((idx >> 1) & 3) + 4 * (idx & 1));
      const float e2 = (float)(1024 + 128 * idx);
      const float eb = (float)(8 * idx);
      float av[3][8], bv[8];
#pragma unroll
      for (int i = 0; i < 4; ++i) {
#define SEL_(v) ((i & 1) ? bfhi(v[i >> 1]) : bflo(v[i >> 1]))
        const float ar = dt * SEL_(vAr), ai = dt * SEL_(vAi), br = dt * SEL_(vBr), bi = dt * SEL_(vBi);
        const float cr = SEL_(vCr), ci = SEL_(vCi);
#undef SEL_
        const cplx cb = {cr * br - ci * bi, cr * bi + ci * br};
        const float arl2 = ar * 1.4426950408889634f;
        const float air = ai * 0.15915494309189535f;
        const cplx A0 = cexp_e(arl2, air, e0), A1 = cexp_e(arl2, air, e1), A2 = cexp_e(arl2, air, e2);
        const cplx B = cmul(cb, cexp_e(arl2, air, eb));
        av[0][2 * i] = A0.re; av[0][2 * i + 1] = A0.im;
        av[1][2 * i] = A1.re; av[1][2 * i + 1] = A1.im;
        av[2][2 * i] = A2.re; av[2][2 * i + 1] = A2.im;
        bv[2 * i] = B.re; bv[2 * i + 1] = -B.im;
      }
      u32x4 ah[3], al[3], bh, bl;
#pragma unroll
      for (int j = 0; j < 4; ++j) {
#pragma unroll
        for (int b = 0; b < 3; ++b) {
          const unsigned h = pack_bf16(av[b][2 * j], av[b][2 * j + 1]);
          ah[b][j] = h;
          al[b][j] = pack_bf16(av[b][2 * j] - bflo(h), av[b][2 * j + 1] - bfhi(h));
        }
        const unsigned h = pack_bf16(bv[2 * j], bv[2 * j + 1]);
        bh[j] = h;
        bl[j] = pack_bf16(bv[2 * j] - bflo(h), bv[2 * j + 1] - bfhi(h));
      }
#pragma unroll
      for (int b = 0; b < 3; ++b) {
        f32x4 ka = {0.f, 0.f, 0.f, 0.f};
        ka = __builtin_amdgcn_mfma_f32_16x16x32_bf16(__builtin_bit_cast(s16x8, al[b]), __builtin_bit_cast(s16x8, bh), ka, 0, 0, 0);
        ka = __builtin_amdgcn_mfma_f32_16x16x32_bf16(__builtin_bit_cast(s16x8, ah[b]), __builtin_bit_cast(s16x8, bl), ka, 0, 0, 0);
        ka = __builtin_amdgcn_mfma_f32_16x16x32_bf16(__builtin_bit_cast(s16x8, ah[b]), __builtin_bit_cast(s16x8, bh), ka, 0, 0, 0);
#pragma unroll
        for (int r = 0; r < 4; ++r) red[(w * 3 + b) * 256 + (4 * kk + r) * 16 + idx] = ka[r];
      }
    }
    __syncthreads();
    for (int e = tid; e < 768; e += 512) {
      float sacc = red[e];
#pragma unroll
      for (int ww = 1; ww < 8; ++ww) sacc += red[ww * 768 + e];
      Kd[e] = sacc;
    }
    __syncthreads();
    {
      float kv[4];
      float mx = 0.f;
#pragma unroll
      for (int h = 0; h < 4; ++h) {
        const int p = 4 * tid + h;
        const int ll = 2047 - p;
        const int lr = (int)rbf((float)ll);
        const int q = (lr >> 3) & 15;
        int row;
        if (lr < 256) row = ((lr >> 7) << 3) + (lr & 7);
        else if (lr < 512) row = 16 + (((lr >> 7) & 1) << 2) + ((lr & 7) >> 1);
        else if (lr < 1024) row = 24 + (((lr >> 7) & 3) << 1) + ((lr & 7) >> 2);
        else row = 32 + ((lr - 1024) >> 7);
        kv[h] = Kd[row * 16 + q];
        mx = fmaxf(mx, fabsf(kv[h]));
      }
#pragma unroll
      for (int o = 1; o < 64; o <<= 1) mx = fmaxf(mx, __shfl_xor(mx, o));
      float* wmx = red;
      if (l == 0) wmx[w] = mx;
      __syncthreads();
      mx = wmx[0];
#pragma unroll
      for (int ww = 1; ww < 8; ++ww) mx = fmaxf(mx, wmx[ww]);
      int ex = (int)((__float_as_uint(mx) >> 23) & 255u);
      int se = 137 - ex;
      se = se > 120 ? 120 : (se < -120 ? -120 : se);
      const float sc = __uint_as_float((unsigned)(127 + se) << 23);
      typedef __attribute__((ext_vector_type(2))) _Float16 h16x2;
      h16x2 p0 = {(_Float16)(kv[0] * sc), (_Float16)(kv[1] * sc)};
      h16x2 p1 = {(_Float16)(kv[2] * sc), (_Float16)(kv[3] * sc)};
      u32x2 o2 = {__builtin_bit_cast(unsigned, p0), __builtin_bit_cast(unsigned, p1)};
      *(u32x2*)(KR + (size_t)d * 2048 + 4 * tid) = o2;
      if (tid == 0) Ksc[d] = __uint_as_float((unsigned)(127 - se) << 23);
    }
    return;
  }
  if (bx < 4) counters[bx * 512 + tid] = 0;
  const int t0 = bx * LN_ROWS;
  const int r = w * 2 + (l >> 5), c = l & 31;
  const bool act_l = c < 24;
  const u32x4* xr = (const u32x4*)(x + (size_t)(t0 + r) * D);
  u32x4 v[3];
#pragma unroll
  for (int i = 0; i < 3; ++i) v[i] = act_l ? xr[c + 24 * i] : (u32x4){0u, 0u, 0u, 0u};
  float s = 0.f;
#pragma unroll
  for (int i = 0; i < 3; ++i)
#pragma unroll
    for (int j = 0; j < 4; ++j) s += bflo(v[i][j]) + bfhi(v[i][j]);
#pragma unroll
  for (int o = 1; o < 32; o <<= 1) s += __shfl_xor(s, o);
  const float mean = s * (1.0f / D);
  float q = 0.f;
#pragma unroll
  for (int i = 0; i < 3; ++i)
#pragma unroll
    for (int j = 0; j < 4; ++j) {
      float a = bflo(v[i][j]) - mean, b = bfhi(v[i][j]) - mean;
      q += a * a + b * b;
    }
  if (!act_l) q = 0.f;
#pragma unroll
  for (int o = 1; o < 32; o <<= 1) q += __shfl_xor(q, o);
  const float var = q * (1.0f / D);
  const float mb = rbf(mean), vb = rbf(var);
  const float den = rbf(sqrtf(rbf(vb + 1e-5f)));
  const float rc = 1.0f / den;
  u32x4* xo = (u32x4*)(xn + (size_t)(t0 + r) * D);
  if (act_l) {
#pragma unroll
    for (int i = 0; i < 3; ++i) {
      u32x4 o;
#pragma unroll
      for (int j = 0; j < 4; ++j) {
        const float da = rbf(bflo(v[i][j]) - mb), db = rbf(bfhi(v[i][j]) - mb);
        float qa = da * rc, qb = db * rc;
        qa = fmaf(fmaf(-qa, den, da), rc, qa);
        qb = fmaf(fmaf(-qb, den, db), rc, qb);
        o[j] = pack_bf16(qa, qb);
      }
      xo[c + 24 * i] = o;
      *(u32x4*)(smem + (r * LN_TS + (c + 24 * i) * 8) * 2) = o;
    }
  }
  __syncthreads();
  const unsigned sbase = (unsigned)(uintptr_t)smem;
  const int g = l >> 4, qq = (l & 15) >> 2, p = l & 3;
  for (int it = w; it < 18; it += 8) {
    const int d0 = it * 32 + (g >> 1) * 16;
    const int rb = (g & 1) * 8;
    s16x4 a = tr_read(sbase + ((rb + qq) * LN_TS + d0 + 4 * p) * 2);
    s16x4 b = tr_read(sbase + ((rb + 4 + qq) * LN_TS + d0 + 4 * p) * 2);
    *(s16x8*)(uT + (size_t)(d0 + (l & 15)) * L + t0 + rb) = cat8(a, b);
  }
}

constexpr int G1_A_BYTES = 128 * 32 * 2;
constexpr int G1_B_BYTES = 32 * 288 * 2;
constexpr int G1_STAGE = G1_A_BYTES + G1_B_BYTES;
constexpr int CV2_UC = 4160;
constexpr int CV2_K = 2 * CV2_UC;
constexpr int CV2_BYTES = CV2_K + 4672 + 32;
constexpr int G1_LDS = 4 * G1_STAGE + 3 * CV2_BYTES;

constexpr int CV_UC_STRIDE = 4160;
constexpr int CV_K_OFF = 4 * CV_UC_STRIDE;
constexpr int CV_K_BYTES = 2336 * 2;
constexpr int CV_KD_OFF = CV_K_OFF + 2 * CV_K_BYTES;
constexpr int CV_CH_STRIDE = 32768;
constexpr int CV_BLOCKS = D / 3;

__device__ __forceinline__ float mish_f(float h) {
  float t = fminf(h * 1.4426950408889634f, 43.f);
  float e = __builtin_amdgcn_exp2f(t);
  float a = e + 2.f;
  float n = e * a;
  float n2 = fmaf(e, a, 2.f);
  return h * (n * __builtin_amdgcn_rcpf(n2));
}

__device__ __forceinline__ unsigned mish2(float h0, float h1) {
  const f32x2 h = {h0, h1};
  f32x2 t = h * 1.4426950408889634f;
  f32x2 e = {__builtin_amdgcn_exp2f(fminf(t[0], 43.f)), __builtin_amdgcn_exp2f(fminf(t[1], 43.f))};
  f32x2 a = e + 2.f;
  f32x2 n = e * a;
  f32x2 n2 = n + 2.f;
  f32x2 r = {__builtin_amdgcn_rcpf(n2[0]), __builtin_amdgcn_rcpf(n2[1])};
  f32x2 o = h * (n * r);
  return pack_bf16(o[0], o[1]);
}

__global__ __launch_bounds__(768, 3) void gemm1_conv_kernel(
    const u16* __restrict__ xn, const u16* __restrict__ w1, const u16* __restrict__ b1, u16* __restrict__ act,
    const u16* __restrict__ uT, const u16* __restrict__ KR, const float* __restrict__ Ksc, u16* __restrict__ yT) {
  extern __shared__ __attribute__((aligned(16))) char smem[];
  const unsigned sbase = (unsigned)(uintptr_t)smem;
  const int tid = threadIdx.x, l = tid & 63;
  const int w = __builtin_amdgcn_readfirstlane(tid >> 6);
  const int g = l >> 4, li = l & 15;
  if (blockIdx.x < 256) {
    const int b = blockIdx.x, xcd = b & 7, idx = b >> 3;
    const int mi = 8 * (xcd & 1) + (idx & 7), ni = 4 * (xcd >> 1) + (idx >> 3);
    const int m0 = mi * 128, n0 = ni * 288;
    if (w >= 8) {
      const int lw = w - 8;
      int aoff[2], boff[5];
#pragma unroll
      for (int j = 0; j < 2; ++j) {
        const int p = (lw + 4 * j) * 64 + l;
        const int row = p >> 2, chp = p & 3;
        const int f = (4 - ((row >> 2) & 3)) & 3;
        aoff[j] = (m0 + row) * D + ((chp ^ f) * 8);
      }
#pragma unroll
      for (int j = 0; j < 5; ++j) {
        int ins = lw + 4 * j;
        if (ins >= 18) ins = 0;
        const int p = ins * 64 + l;
        const int row = p / 36, chp = p - row * 36;
        boff[j] = row * F + n0 + ((chp ^ (((row >> 3) & 1) << 1)) * 8);
      }
      auto stage = [&](int t, int buf) {
        const unsigned sb = sbase + buf * G1_STAGE;
#pragma unroll
        for (int j = 0; j < 2; ++j) glds16(xn + aoff[j] + t * 32, sb + (lw + 4 * j) * 1024);
#pragma unroll
        for (int j = 0; j < 4; ++j) glds16(w1 + boff[j] + (size_t)t * 32 * F, sb + G1_A_BYTES + (lw + 4 * j) * 1024);
        if (lw < 2) glds16(w1 + boff[4] + (size_t)t * 32 * F, sb + G1_A_BYTES + (lw + 16) * 1024);
      };
#define LVMWAIT(big, small) do { if (lw < 2) asm volatile("s_waitcnt vmcnt(" #big ")" ::: "memory"); else asm volatile("s_waitcnt vmcnt(" #small ")" ::: "memory"); } while (0)
      typedef __attribute__((ext_vector_type(8))) _Float16 h16x8;
      typedef __attribute__((ext_vector_type(2))) _Float16 h16x2;
      __builtin_amdgcn_s_setprio(1);
      const int d = b + 256 * lw;
      const bool hasc = d < D;
      const int coff = 4 * G1_STAGE + lw * CV2_BYTES;
      char* cs = smem + coff;
      const unsigned cbase = sbase + coff;
      float ysc = 0.f;
      {
        u32x4 cu[4], ck[4];
        unsigned cun[4];
        if (hasc) {
#pragma unroll
          for (int i = 0; i < 4; ++i) {
            const int e = 8 * (l + 64 * i);
            cu[i] = *(const u32x4*)(uT + (size_t)d * L + e);
            cun[i] = (e + 8 < L) ? *(const unsigned*)(uT + (size_t)d * L + e + 8) : 0u;
            ck[i] = *(const u32x4*)(KR + (size_t)d * 2048 + e);
          }
          ysc = Ksc[d];
        }
        stage(0, 0);
        stage(1, 1);
        stage(2, 2);
        if (hasc) {
          LVMWAIT(21, 18);
          asm volatile("" : "+v"(cu[0]), "+v"(cu[1]), "+v"(cu[2]), "+v"(cu[3]), "+v"(ck[0]), "+v"(ck[1]), "+v"(ck[2]), "+v"(ck[3]),
                       "+v"(cun[0]), "+v"(cun[1]), "+v"(cun[2]), "+v"(cun[3]), "+v"(ysc));
#pragma unroll
          for (int i = 0; i < 4; ++i) {
            const int tc = l + 64 * i;
            *(u32x4*)(cs + CV2_K + tc * 16) = ck[i];
            unsigned wv[5];
#pragma unroll
            for (int j = 0; j < 5; ++j) {
              const unsigned src = j < 4 ? cu[i][j] : cun[i];
              h16x2 hv = {(_Float16)bflo(src), (_Float16)bfhi(src)};
              wv[j] = __builtin_bit_cast(unsigned, hv);
            }
            u32x4 c0 = {wv[0], wv[1], wv[2], wv[3]}, c1;
#pragma unroll
            for (int j = 0; j < 4; ++j) c1[j] = __builtin_amdgcn_alignbyte(wv[j + 1], wv[j], 2);
            char* dst = cs + (8 * tc + 16) * 2;
            *(u32x4*)(dst) = c0;
            *(u32x4*)(dst + CV2_UC) = c1;
            if (i == 0) {
              const unsigned h0 = __shfl(wv[0], 0);
              if (l < 32) {
                const int c = l >> 4, ii = l & 15;
                *(u16*)(cs + c * CV2_UC + ii * 2) = (u16)((c == 1 && ii == 15) ? (h0 & 0xffffu) : 0u);
              }
            }
          }
          if (l < 36) *(u32x4*)(cs + CV2_K + 4096 + l * 16) = (u32x4){0u, 0u, 0u, 0u};
        }
      }
      f32x4 cacc[8];
#pragma unroll
      for (int j = 0; j < 8; ++j) cacc[j] = (f32x4){0.f, 0.f, 0.f, 0.f};
      const int cc = (li + 1) & 1;
      const unsigned ua = cbase + cc * CV2_UC + (li + 1 - cc + 8 * g) * 2;
      const unsigned kb = cbase + CV2_K + (2032 - 16 * li + 8 * g) * 2;
      LVMWAIT(14, 12);
      __builtin_amdgcn_s_barrier();
      for (int rho = 0; rho < 8; ++rho) {
        const int t = 2 * rho;
        u32x4 A[8];
        s16x8 Bh[8];
        LVMWAIT(7, 6);
        __builtin_amdgcn_s_barrier();
        stage(t + 3, (t + 3) & 3);
        if (hasc) {
#pragma unroll
          for (int s = 0; s < 8; ++s) {
            const unsigned ad = ua + (32 * (8 * s + rho)) * 2;
#pragma unroll
            for (int j = 0; j < 4; ++j) A[s][j] = *(LDS_AS unsigned*)(uintptr_t)(ad + 4 * j);
          }
#pragma unroll
          for (int dl = 0; dl < 8; ++dl) Bh[dl] = lds_read128(kb + (32 * rho - 256 * dl) * 2);
#pragma unroll
          for (int j = 0; j < 6; ++j)
#pragma unroll
            for (int s = 0; s <= j; ++s)
              cacc[j] = __builtin_amdgcn_mfma_f32_16x16x32_f16(__builtin_bit_cast(h16x8, A[s]), __builtin_bit_cast(h16x8, Bh[j - s]), cacc[j], 0, 0, 0);
        }
        LVMWAIT(7, 6);
        __builtin_amdgcn_s_barrier();
        if (t + 4 < 18) stage(t + 4, (t + 4) & 3);
        if (hasc) {
#pragma unroll
          for (int j = 6; j < 8; ++j)
#pragma unroll
            for (int s = 0; s <= j; ++s)
              cacc[j] = __builtin_amdgcn_mfma_f32_16x16x32_f16(__builtin_bit_cast(h16x8, A[s]), __builtin_bit_cast(h16x8, Bh[j - s]), cacc[j], 0, 0, 0);
        }
      }
      asm volatile("s_waitcnt vmcnt(0)" ::: "memory");
      __builtin_amdgcn_s_barrier();
      if (hasc) {
#pragma unroll
        for (int j = 0; j < 8; ++j) {
          const f32x4 sv = cacc[j] * ysc;
          u32x2 o = {pack_bf16(sv[0], sv[1]), pack_bf16(sv[2], sv[3])};
          *(u32x2*)(yT + (size_t)d * L + 256 * j + 16 * li + 4 * g) = o;
        }
      }
    } else {
      const int wm = w & 3, wn = w >> 2;
      const int fa = (4 - ((li >> 2) & 3)) & 3;
      const unsigned a_rd = (32 * wm + li) * 64 + ((g ^ fa) * 16);
      const int q = li >> 2, p4 = li & 3;
      const unsigned b_rd = G1_A_BYTES + (8 * g + q) * 576 + (p4 & 1) * 8;
      const int bsw = (g & 1) << 1;
      f32x4 acc[9][2];
#pragma unroll
      for (int nt = 0; nt < 9; ++nt) {
        const u32x2 bb = *(const u32x2*)(b1 + n0 + wn * 144 + nt * 16 + 4 * g);
        const f32x4 bv = {bflo(bb[0]), bfhi(bb[0]), bflo(bb[1]), bfhi(bb[1])};
#pragma unroll
        for (int mt = 0; mt < 2; ++mt) acc[nt][mt] = bv;
      }
      const unsigned bE0 = b_rd + (p4 >> 1) * 16 + wn * 288 + (wn ? -bsw * 16 : bsw * 16);
      const unsigned bO0 = b_rd + (p4 >> 1) * 16 + wn * 288 + (wn ? bsw * 16 : -bsw * 16);
      s16x8 xa0[2], xa1[2];
      s16x4 wl0[9], wh0[9], wl1[9], wh1[9];
      __builtin_amdgcn_s_barrier();
      {
        const unsigned pA = sbase + a_rd, pE = sbase + bE0, pO = sbase + bO0;
        RDSET(xa0, wl0, wh0, pA, 1024, pE, pO);
      }
      for (int t = 0; t < 16; t += 2) {
        {
          __builtin_amdgcn_s_barrier();
          const unsigned sbn = sbase + ((t + 1) & 3) * G1_STAGE;
          const unsigned pA = sbn + a_rd, pE = sbn + bE0, pO = sbn + bO0;
          WAITSET(0, xa0, wl0, wh0);
          RDSET(xa1, wl1, wh1, pA, 1024, pE, pO);
          MMASET(xa0, wl0, wh0);
        }
        {
          __builtin_amdgcn_s_barrier();
          const unsigned sbn = sbase + ((t + 2) & 3) * G1_STAGE;
          const unsigned pA = sbn + a_rd, pE = sbn + bE0, pO = sbn + bO0;
          WAITSET(0, xa1, wl1, wh1);
          RDSET(xa0, wl0, wh0, pA, 1024, pE, pO);
          MMASET(xa1, wl1, wh1);
        }
      }
      {
        __builtin_amdgcn_s_barrier();
        const unsigned sbn = sbase + (17 & 3) * G1_STAGE;
        const unsigned pA = sbn + a_rd, pE = sbn + bE0, pO = sbn + bO0;
        WAITSET(0, xa0, wl0, wh0);
        RDSET(xa1, wl1, wh1, pA, 1024, pE, pO);
        MMASET(xa0, wl0, wh0);
        WAITSET(0, xa1, wl1, wh1);
        MMASET(xa1, wl1, wh1);
      }
      int tz = threadIdx.x;
      asm volatile("" : "+v"(tz));
      const int li2 = tz & 15, g2 = (tz >> 4) & 3;
#pragma unroll
      for (int mt = 0; mt < 2; ++mt) {
        const int row = 32 * wm + 16 * mt + li2;
        const int sw = (row >> 1) & 7;
#pragma unroll
        for (int nt = 0; nt < 9; ++nt) {
          const int n = n0 + wn * 144 + nt * 16 + 4 * g2;
          const int kt = n >> 6, ch = (n >> 3) & 7;
          u16* dst = act + ((size_t)(mi * 72 + kt) * 128 + row) * 64 + ((ch ^ sw) * 8) + (n & 7);
          u32x2 o;
          o[0] = mish2(acc[nt][mt][0], acc[nt][mt][1]);
          o[1] = mish2(acc[nt][mt][2], acc[nt][mt][3]);
          *(u32x2*)(dst) = o;
          if (nt % 3 == 2) __builtin_amdgcn_sched_barrier(0);
        }
      }
    }
  }
}

constexpr int G2_A_BYTES = 128 * 64 * 2;
constexpr int G2_B_BYTES = 32 * 576;
constexpr int G2_STAGE = G2_A_BYTES + G2_B_BYTES;
constexpr int G2_NST = 4;
constexpr int G2_LDS = G2_NST * G2_STAGE;

__global__ __launch_bounds__(768, 3) void gemm2_kernel(
    const u16* __restrict__ act, const u16* __restrict__ w2, float* __restrict__ slabs, int* __restrict__ counters,
    const u16* __restrict__ x, const u16* __restrict__ xn, const u16* __restrict__ yT, const u16* __restrict__ Dskip,
    u16* __restrict__ out) {
  extern __shared__ __attribute__((aligned(16))) char smem[];
  const unsigned sbase = (unsigned)(uintptr_t)smem;
  const int tid = threadIdx.x, l = tid & 63;
  const int w = __builtin_amdgcn_readfirstlane(tid >> 6);
  const int g = l >> 4, li = l & 15;
  const int b = blockIdx.x, xcd = b & 7, idx = b >> 3;
  const int mi = 8 * (xcd & 1) + (idx & 7), ks = xcd >> 1, ni = idx >> 3;
  const int m0 = mi * 128, n0 = ni * 144, k0 = ks * 1152;
  const int wm = w & 3, wk = (w >> 2) & 1;
  f32x4 acc[9][2];
#pragma unroll
  for (int nt = 0; nt < 9; ++nt)
#pragma unroll
    for (int mt = 0; mt < 2; ++mt) acc[nt][mt] = (f32x4){0.f, 0.f, 0.f, 0.f};
  const int R0 = m0 + 32 * ks;
  const int uh = w >= 5 ? 1 : 0, uj = w - 5 * uh;
  const int ntile = (w >= 10) ? 0 : (uj < 4 ? 2 : 1);
  u32x2 xv[2], nv[2], dv[2];
  unsigned yv[2][4];
  auto load_ops = [&]() {
#pragma unroll
    for (int i = 0; i < 2; ++i) {
      int nt = 2 * uj + i;
      nt = nt > 8 ? 8 : nt;
      const int row = R0 + 16 * uh + li, col = n0 + nt * 16 + 4 * g;
      const size_t go = (size_t)row * D + col;
      xv[i] = *(const u32x2*)(x + go);
      nv[i] = *(const u32x2*)(xn + go);
      dv[i] = *(const u32x2*)(Dskip + col);
#pragma unroll
      for (int e = 0; e < 4; ++e) yv[i][e] = yT[(size_t)(col + e) * L + row];
    }
  };
  if (w >= 8) {
    const int lw = w - 8;
    const u16* aptr = act + (size_t)(mi * 72 + ks * 18) * 8192 + (lw * 64 + l) * 8;
    int boff[5];
#pragma unroll
    for (int j = 0; j < 5; ++j) {
      int ins = lw + 4 * j;
      if (ins >= 18) ins = 0;
      const int p = ins * 64 + l;
      const int row = p / 36, chp = p - row * 36;
      const int ch = chp ^ (((row >> 3) & 1) << 1);
      const int half = ch >= 18 ? 1 : 0;
      boff[j] = (k0 + row + 32 * half) * D + n0 + (ch - 18 * half) * 8;
    }
    auto stage = [&](int t, int buf) {
      const unsigned sb = sbase + buf * G2_STAGE;
#pragma unroll
      for (int j = 0; j < 4; ++j) glds16(aptr + t * 8192 + j * 2048, sb + (lw + 4 * j) * 1024);
#pragma unroll
      for (int j = 0; j < 4; ++j) glds16(w2 + boff[j] + t * 64 * D, sb + G2_A_BYTES + (lw + 4 * j) * 1024);
      if (lw < 2) glds16(w2 + boff[4] + t * 64 * D, sb + G2_A_BYTES + (lw + 16) * 1024);
    };
    stage(0, 0);
    stage(1, 1);
    stage(2, 2);
    LVMWAIT(18, 16);
    __builtin_amdgcn_s_barrier();
    for (int t = 0; t < 15; ++t) {
      LVMWAIT(9, 8);
      __builtin_amdgcn_s_barrier();
      stage(t + 3, (t + 3) & 3);
    }
    asm volatile("" ::: "memory");
    load_ops();
    asm volatile("" ::: "memory");
    LVMWAIT(23, 22);
    __builtin_amdgcn_s_barrier();
    asm volatile("s_waitcnt vmcnt(14)" ::: "memory");
    __builtin_amdgcn_s_barrier();
  } else {
    const unsigned a_rd = (32 * wm + li) * 128 + (((4 * wk + g) ^ ((li >> 1) & 7)) * 16);
    const int q = li >> 2, p4 = li & 3;
    const unsigned b_rd = G2_A_BYTES + (8 * g + q) * 576 + (p4 & 1) * 8;
    const int bsw = (g & 1) << 1;
    const unsigned bE0 = b_rd + (p4 >> 1) * 16 + wk * 288 + (wk ? -bsw * 16 : bsw * 16);
    const unsigned bO0 = b_rd + (p4 >> 1) * 16 + wk * 288 + (wk ? bsw * 16 : -bsw * 16);
    s16x8 xa0[2], xa1[2];
    s16x4 wl0[9], wh0[9], wl1[9], wh1[9];
    __builtin_amdgcn_s_barrier();
    {
      const unsigned pA = sbase + a_rd, pE = sbase + bE0, pO = sbase + bO0;
      RDSET(xa0, wl0, wh0, pA, 2048, pE, pO);
    }
    asm volatile("" ::: "memory");
    load_ops();
    asm volatile("" ::: "memory");
    for (int t = 0; t < 16; t += 2) {
      {
        __builtin_amdgcn_s_barrier();
        const unsigned sbn = sbase + ((t + 1) & 3) * G2_STAGE;
        const unsigned pA = sbn + a_rd, pE = sbn + bE0, pO = sbn + bO0;
        WAITSET(0, xa0, wl0, wh0);
        RDSET(xa1, wl1, wh1, pA, 2048, pE, pO);
        MMASET(xa0, wl0, wh0);
      }
      {
        __builtin_amdgcn_s_barrier();
        const unsigned sbn = sbase + ((t + 2) & 3) * G2_STAGE;
        const unsigned pA = sbn + a_rd, pE = sbn + bE0, pO = sbn + bO0;
        WAITSET(0, xa1, wl1, wh1);
        RDSET(xa0, wl0, wh0, pA, 2048, pE, pO);
        MMASET(xa1, wl1, wh1);
      }
    }
    {
      __builtin_amdgcn_s_barrier();
      const unsigned sbn = sbase + (17 & 3) * G2_STAGE;
      const unsigned pA = sbn + a_rd, pE = sbn + bE0, pO = sbn + bO0;
      WAITSET(0, xa0, wl0, wh0);
      RDSET(xa1, wl1, wh1, pA, 2048, pE, pO);
      MMASET(xa0, wl0, wh0);
      WAITSET(0, xa1, wl1, wh1);
      MMASET(xa1, wl1, wh1);
    }
  }
  __syncthreads();
  f32x4 res[9];
  {
    f32x4* ex = (f32x4*)smem;
    if (w < 4) {
#pragma unroll
      for (int nt = 0; nt < 9; ++nt) ex[(w * 9 + nt) * 64 + l] = acc[nt][1];
    } else if (w < 8) {
#pragma unroll
      for (int nt = 0; nt < 9; ++nt) ex[(w * 9 + nt) * 64 + l] = acc[nt][0];
    }
    __syncthreads();
    const int pw = (w ^ 4) & 7;
    if (w < 4) {
#pragma unroll
      for (int nt = 0; nt < 9; ++nt) res[nt] = acc[nt][0] + ex[(pw * 9 + nt) * 64 + l];
    } else {
#pragma unroll
      for (int nt = 0; nt < 9; ++nt) res[nt] = acc[nt][1] + ex[(pw * 9 + nt) * 64 + l];
    }
  }
  const int grp = mi * 4 + ni;
  {
    float* gslab = slabs + (size_t)grp * (147456 / 4);
    auto rs = __builtin_amdgcn_make_buffer_rsrc((void*)gslab, 0, 147456, 0x00020000);
    __syncthreads();
    f32x4* own = (f32x4*)smem;
    int* flags = counters + grp * 32;
    if (w >= 8) {
    } else if (wm == ks) {
#pragma unroll
      for (int nt = 0; nt < 9; ++nt) own[(wk * 9 + nt) * 64 + l] = res[nt];
    } else {
      const int base = ((wm * 4 + ks) * 2 + wk) * 4608;
#pragma unroll
      for (int j = 0; j < 4; ++j) {
        const u32x4 pk = {pack_bf16(res[2 * j][0], res[2 * j][1]), pack_bf16(res[2 * j][2], res[2 * j][3]),
                          pack_bf16(res[2 * j + 1][0], res[2 * j + 1][1]), pack_bf16(res[2 * j + 1][2], res[2 * j + 1][3])};
        __builtin_amdgcn_raw_buffer_store_b128(pk, rs, base + j * 1024 + l * 16, 0, 16);
      }
      const u32x2 pk8 = {pack_bf16(res[8][0], res[8][1]), pack_bf16(res[8][2], res[8][3])};
      __builtin_amdgcn_raw_buffer_store_b64(pk8, rs, base + 4096 + l * 8, 0, 16);
      asm volatile("s_waitcnt vmcnt(0)" ::: "memory");
      if (l == 0) __hip_atomic_store(&flags[(wm * 4 + ks) * 2 + wk], 1, __ATOMIC_RELAXED, __HIP_MEMORY_SCOPE_AGENT);
    }
    asm volatile("s_waitcnt lgkmcnt(0)" ::: "memory");
    __builtin_amdgcn_s_barrier();
    if (ntile > 0) {
      {
        const int src = l & 3;
        const bool need = (l < 4) && (src != ks);
        for (int spin = 0; spin < (1 << 20); ++spin) {
          int v = 1;
          if (need) v = __hip_atomic_load(&flags[(ks * 4 + src) * 2 + uh], __ATOMIC_RELAXED, __HIP_MEMORY_SCOPE_AGENT);
          if (__all(v != 0)) break;
          __builtin_amdgcn_s_sleep(1);
        }
        asm volatile("" ::: "memory");
      }
      u32x4 pv[3];
#pragma unroll
      for (int k = 0; k < 3; ++k) {
        const int src = k + (k >= ks ? 1 : 0);
        const int base = ((ks * 4 + src) * 2 + uh) * 4608;
        if (uj < 4) pv[k] = __builtin_amdgcn_raw_buffer_load_b128(rs, base + uj * 1024 + l * 16, 0, 16);
        else { const u32x2 t2 = __builtin_amdgcn_raw_buffer_load_b64(rs, base + 4096 + l * 8, 0, 16); pv[k] = (u32x4){t2[0], t2[1], 0u, 0u}; }
      }
#pragma unroll
      for (int i = 0; i < 2; ++i) {
        if (i < ntile) {
          const int nt = 2 * uj + i;
          const int row = R0 + 16 * uh + li, col = n0 + nt * 16 + 4 * g;
          f32x4 parts[4];
          const f32x4 mine = own[(uh * 9 + nt) * 64 + l];
#pragma unroll
          for (int k = 0; k < 4; ++k) {
            const int kk = k - (k > ks ? 1 : 0);
            const u32x4 pq = pv[kk < 3 ? kk : 2];
            const f32x4 rem = {bflo(pq[2 * i]), bfhi(pq[2 * i]), bflo(pq[2 * i + 1]), bfhi(pq[2 * i + 1])};
            parts[k] = (k == ks) ? mine : rem;
          }
          const f32x4 sum = (parts[0] + parts[1]) + (parts[2] + parts[3]);
          float o[4];
#pragma unroll
          for (int e = 0; e < 4; ++e) {
            const float xf = (e & 1) ? bfhi(xv[i][e >> 1]) : bflo(xv[i][e >> 1]);
            const float nf = (e & 1) ? bfhi(nv[i][e >> 1]) : bflo(nv[i][e >> 1]);
            const float df = (e & 1) ? bfhi(dv[i][e >> 1]) : bflo(dv[i][e >> 1]);
            const float ya = rbf(bflo(yv[i][e]) + rbf(nf * df));
            o[e] = rbf(xf + ya) + rbf(sum[e]);
          }
          u32x2 ov = {pack_bf16(o[0], o[1]), pack_bf16(o[2], o[3])};
          *(u32x2*)(out + (size_t)row * D + col) = ov;
        }
      }
    }
  }
}

extern "C" void kernel_launch(void* const* d_in, const int* in_sizes, int n_in,
                              void* d_out, int out_size, void* d_ws, size_t ws_size,
                              hipStream_t stream) {
  const u16* x = (const u16*)d_in[0];
  const u16* Are = (const u16*)d_in[1];
  const u16* Aim = (const u16*)d_in[2];
  const u16* Bre = (const u16*)d_in[3];
  const u16* Bim = (const u16*)d_in[4];
  const u16* Cre = (const u16*)d_in[5];
  const u16* Cim = (const u16*)d_in[6];
  const u16* logdt = (const u16*)d_in[7];
  const u16* Dskip = (const u16*)d_in[8];
  const u16* w1 = (const u16*)d_in[10];
  const u16* b1 = (const u16*)d_in[11];
  const u16* w2 = (const u16*)d_in[12];
  u16* out = (u16*)d_out;
  char* ws = (char*)d_ws;
  constexpr size_t SZ = (size_t)L * D * 2;
  int* counters = (int*)(ws);
  float* Ksc = (float*)(ws + 16384);
  unsigned* sinkp = (unsigned*)(ws + 32768);
  u16* xn = (u16*)(ws + 65536);
  u16* uT = (u16*)(ws + 65536 + SZ);
  u16* yT = (u16*)(ws + 65536 + 2 * SZ);
  u16* KR = (u16*)(ws + 65536 + 3 * SZ);
  float* slabs = (float*)(ws + 65536 + 4 * SZ);
  u16* act = (u16*)(ws + 65536 + 4 * SZ + 64 * 147456);
  hipFuncSetAttribute((const void*)gemm1_conv_kernel, hipFuncAttributeMaxDynamicSharedMemorySize, G1_LDS);
  hipFuncSetAttribute((const void*)gemm2_kernel, hipFuncAttributeMaxDynamicSharedMemorySize, G2_LDS);
  ln_kernel<<<LN_BLOCKS + D + 256, 512, 0, stream>>>(x, xn, uT, counters, Are, Aim, Bre, Bim, Cre, Cim, logdt, KR, Ksc, w1, w2, sinkp);
  gemm1_conv_kernel<<<256, 768, G1_LDS, stream>>>(xn, w1, b1, act, uT, KR, Ksc, yT);
  gemm2_kernel<<<256, 768, G2_LDS, stream>>>(act, w2, slabs, counters, x, xn, yT, Dskip, out);
}
```
